```python
import torch

def _softmax(x, axis=-1):
    return torch.softmax(x, dim=axis)

def _gelu(x):
    return torch.nn.functional.gelu(x, approximate='tanh')

def _silu(x):
    return torch.nn.functional.silu(x)

def _sigmoid(x):
    return torch.sigmoid(x)

def _layernorm(x, axis=-1, eps=1e-05):
    m = x.mean(dim=axis, keepdim=True)
    v = x.var(dim=axis, keepdim=True, unbiased=False)
    return (x - m) / torch.sqrt(v + eps)

def _scatter_add(tgt, idx, src, axis=0):
    return tgt.clone().index_add_(axis, idx, src)

def _segment_sum(src, seg, n):
    out = torch.zeros((n,) + src.shape[1:], dtype=src.dtype)
    return out.index_add_(0, seg, src)

def _pad(x, pw):
    flat = tuple((v for pair in reversed(pw) for v in pair))
    return torch.nn.functional.pad(x, flat)
kernel_name = 'parallel_sum__linear_kernel_hedgehog__mixer_gmlp__prompt'

def setup_inputs():

    _w = lambda a: (lambda t: t.to(torch.bfloat16) if t.is_floating_point() else t)(torch.as_tensor(a))

    def _A_setup():
        B, S, D, H = (1, 2048, 448, 16)
        Dh = D // H
        return {'x': _w((lambda *s: torch.randn(*s, dtype=torch.bfloat16) * 0.05)(B, S, D)), 'wqkv': _w((lambda *s: torch.randn(*s, dtype=torch.bfloat16) * 0.8)(D, 3 * D)), 'w_phi': _w((lambda *s: torch.randn(*s, dtype=torch.bfloat16) * 0.8)(H, Dh, Dh) * Dh ** (-0.5)), 'wo': _w((lambda *s: torch.randn(*s, dtype=torch.bfloat16) * 0.1)(D, D))}
    _A_in = _A_setup()

    def _B_setup():
        B, S, D, F = (1, 2048, 448, 1792)
        Fh = F // 2
        return {'x': _w((lambda *s: torch.randn(*s, dtype=torch.bfloat16) * 0.05)(B, S, D)), 'w_in': _w((lambda *s: torch.randn(*s, dtype=torch.bfloat16) * 0.4)(D, 2 * Fh)), 'w_sp': _w(0.01 * (lambda *s: torch.randn(*s, dtype=torch.bfloat16) * 0.4)(S, S)), 'b_sp': _w(1.0 + 0.01 * (lambda *s: torch.randn(*s, dtype=torch.bfloat16) * 0.05)(S)), 'w_out': _w((lambda *s: torch.randn(*s, dtype=torch.bfloat16) * 0.05)(Fh, D))}
    _B_in = _B_setup()
    return {'A_x': _A_in['x'], 'A_wqkv': _A_in['wqkv'], 'A_w_phi': _A_in['w_phi'], 'A_wo': _A_in['wo'], 'B_x': _B_in['x'], 'B_w_in': _B_in['w_in'], 'B_w_sp': _B_in['w_sp'], 'B_b_sp': _B_in['b_sp'], 'B_w_out': _B_in['w_out']}

def reference(A_x, A_wqkv, A_w_phi, A_wo, B_x, B_w_in, B_w_sp, B_b_sp, B_w_out):

    def _A_ref(x, wqkv, w_phi, wo):
        B, S, D = x.shape
        H = 16
        Dh = D // H
        qkv = (lambda x, *d: x.permute(*d))((x @ wqkv).reshape(B, S, 3, H, Dh), 2, 0, 3, 1, 4)
        q, k, v = (qkv[0], qkv[1], qkv[2])
        pq = torch.einsum('bhsd,hdf->bhsf', q, w_phi)
        pk = torch.einsum('bhsd,hdf->bhsf', k, w_phi)
        phi_q = torch.concatenate([_softmax(pq, axis=-1), _softmax(-pq, axis=-1)], axis=-1)
        phi_k = torch.concatenate([_softmax(pk, axis=-1), _softmax(-pk, axis=-1)], axis=-1)
        kv = torch.einsum('bhsf,bhsd->bhfd', phi_k, v)
        z = torch.einsum('bhsf->bhf', phi_k)
        num = torch.einsum('bhqf,bhfd->bhqd', phi_q, kv)
        den = torch.einsum('bhqf,bhf->bhq', phi_q, z)[..., None] + 1e-06
        o = num / den
        return (lambda x, *d: x.permute(*d))(o, 0, 2, 1, 3).reshape(B, S, D) @ wo

    def _B_ref(x, w_in, w_sp, b_sp, w_out):
        B, S, D = x.shape
        F = w_in.shape[-1]
        Fh = F // 2
        z = _gelu(torch.einsum('bsd,df->bsf', x, w_in))
        z1 = z[..., :Fh].clone()
        z2 = _layernorm(z[..., Fh:], axis=-1)
        g = torch.einsum('ts,bsf->btf', w_sp, z2) + b_sp[None, :, None]
        return torch.einsum('bsf,fd->bsd', z1 * g, w_out)
    x = A_x
    xn = _layernorm(x, axis=-1)
    return x + _A_ref(xn, A_wqkv, A_w_phi, A_wo) + _B_ref(xn, B_w_in, B_w_sp, B_b_sp, B_w_out)
```

```cpp
#include <hip/hip_runtime.h>
#include <hip/hip_bf16.h>
#include <stdint.h>

#define DI __device__ __forceinline__
typedef __attribute__((ext_vector_type(8))) __bf16 bf16x8;
typedef __attribute__((ext_vector_type(2))) __bf16 bf16x2;
typedef __attribute__((ext_vector_type(4))) short s16x4;
typedef __attribute__((ext_vector_type(4))) float f32x4;
typedef __attribute__((ext_vector_type(2))) float f32x2;
typedef __attribute__((ext_vector_type(16))) float f32x16;
typedef __attribute__((ext_vector_type(4))) unsigned u32x4;
typedef __attribute__((ext_vector_type(2))) unsigned u32x2;
typedef __attribute__((address_space(3))) char lds_char;
typedef __attribute__((address_space(3))) s16x4 lds_s16x4;

constexpr int S_ = 2048, D_ = 448;

constexpr size_t WS_PHIQ = 0;
constexpr size_t WS_KVS = 4u << 20;
constexpr size_t WS_Z1B = 6u << 20;
constexpr size_t WS_Z2B = 10u << 20;
constexpr size_t WS_STAT = 14u << 20;
constexpr size_t WS_HO = 15u << 20;

DI unsigned pack2(float a, float b) {
  f32x2 v = {a, b};
  return __builtin_bit_cast(unsigned, __builtin_convertvector(v, bf16x2));
}
DI float bf_lo(unsigned u) { return __uint_as_float(u << 16); }
DI float bf_hi(unsigned u) { return __uint_as_float(u & 0xffff0000u); }
DI float rbf(float x) { return bf_lo(pack2(x, 0.f)); }
DI float dot2(unsigned a, unsigned b, float c) {
  return __builtin_amdgcn_fdot2_f32_bf16(__builtin_bit_cast(bf16x2, a), __builtin_bit_cast(bf16x2, b), c, false);
}
DI f32x4 mfma16(u32x4 a, u32x4 b, f32x4 c) {
  return __builtin_amdgcn_mfma_f32_16x16x32_bf16(__builtin_bit_cast(bf16x8, a), __builtin_bit_cast(bf16x8, b), c, 0, 0, 0);
}
DI f32x16 mfma32(u32x4 a, u32x4 b, f32x16 c) {
  return __builtin_amdgcn_mfma_f32_32x32x16_bf16(__builtin_bit_cast(bf16x8, a), __builtin_bit_cast(bf16x8, b), c, 0, 0, 0);
}
DI u32x2 tr_read(lds_char* p) {
  s16x4 r = __builtin_amdgcn_ds_read_tr16_b64_v4i16((lds_s16x4*)p);
  return __builtin_bit_cast(u32x2, r);
}
DI u32x4 tr_read2(lds_char* p0, lds_char* p1) {
  u32x2 a = tr_read(p0), b = tr_read(p1);
  u32x4 r = {a[0], a[1], b[0], b[1]};
  return r;
}
DI float fexp(float x) { return __builtin_amdgcn_exp2f(x * 1.4426950408889634f); }
DI float frcp(float x) { return __builtin_amdgcn_rcpf(x); }
DI float gelu_tanh(float x) {
  float u = 0.7978845608028654f * (x + 0.044715f * x * x * x);
  float e = __builtin_amdgcn_exp2f(-2.885390081777927f * u);
  return x * frcp(1.f + e);
}
DI void glds16(const void* sbase, unsigned voff, unsigned lds_dst) {
  asm volatile("s_mov_b32 m0, %2\n\ts_nop 0\n\tglobal_load_lds_dwordx4 %0, %1"
               :: "v"(voff), "s"(sbase), "s"(lds_dst) : "memory");
}
#define STAMP(kid, i)
#define WAIT_VMI(n) asm volatile("s_waitcnt vmcnt(%0)" :: "i"(n) : "memory")
#define WAIT_VM(n) asm volatile("s_waitcnt vmcnt(" #n ")" ::: "memory")
#define WAIT_LGKM0() asm volatile("s_waitcnt lgkmcnt(0)" ::: "memory")
#define BARRIER() __builtin_amdgcn_s_barrier()

constexpr int K1_WST = 32768;
constexpr int K1_XOFF = 98304;
constexpr int K1_XST = 16384;
constexpr int K1_WPHI = 131072;
constexpr int K1_CS = K1_WPHI + 13312;
constexpr int K1_RT = K1_CS + 1024;
constexpr int K1_MT = K1_RT + 512;
constexpr int K1_ST2 = K1_MT + 512;
constexpr int K1_LDS = 147456 + 2048;

enum { T_Q = 0, T_KV = 1, T_Z1 = 2, T_Z2 = 3 };

typedef __attribute__((address_space(3))) float lds_f;
typedef __attribute__((address_space(3))) f32x4 lds_f4;
typedef __attribute__((address_space(3))) f32x2 lds_f2;
typedef __attribute__((address_space(3))) u32x4 lds_u4;
typedef __attribute__((address_space(3))) u32x2 lds_u2;

DI float amax(float a, float b) { float r; asm("v_max_f32 %0, %1, %2" : "=v"(r) : "v"(a), "v"(b)); return r; }
DI float amin(float a, float b) { float r; asm("v_min_f32 %0, %1, %2" : "=v"(r) : "v"(a), "v"(b)); return r; }
#define GRP_RED(x, OP) do { \
    auto r_ = __builtin_amdgcn_permlane16_swap(__float_as_uint(x), __float_as_uint(x), false, false); \
    x = OP(__uint_as_float(r_[0]), __uint_as_float(r_[1])); \
    auto q_ = __builtin_amdgcn_permlane32_swap(__float_as_uint(x), __float_as_uint(x), false, false); \
    x = OP(__uint_as_float(q_[0]), __uint_as_float(q_[1])); } while (0)
DI float aadd(float a, float b) { return a + b; }
DI float smul(float a, float b) { float r; asm("v_mul_f32 %0, %1, %2" : "=v"(r) : "v"(a), "v"(b)); return r; }
DI f32x2 pk_fma(f32x2 a, f32x2 b, f32x2 c) { return __builtin_elementwise_fma(a, b, c); }
DI f32x2 lo2(const f32x4& v) { return f32x2{v[0], v[1]}; }
DI f32x2 hi2(const f32x4& v) { return f32x2{v[2], v[3]}; }
DI unsigned pack2v(f32x2 v) { return __builtin_bit_cast(unsigned, __builtin_convertvector(v, bf16x2)); }
DI f32x2 unpack2(unsigned u) { return f32x2{bf_lo(u), bf_hi(u)}; }
DI f32x2 gelu2(f32x2 v) {
  const f32x2 c1 = {-2.302208198f, -2.302208198f};
  const f32x2 c2 = {-0.1029432397f, -0.1029432397f};
  f32x2 u = v * pk_fma(v * v, c2, c1);
  f32x2 d = f32x2{__builtin_amdgcn_exp2f(u[0]), __builtin_amdgcn_exp2f(u[1])} + f32x2{1.f, 1.f};
  return v * f32x2{frcp(d[0]), frcp(d[1])};
}
DI float row_sum16(float x) {
  x += __builtin_bit_cast(float, __builtin_amdgcn_update_dpp(0, __builtin_bit_cast(int, x), 0x128, 0xf, 0xf, false));
  x += __builtin_bit_cast(float, __builtin_amdgcn_update_dpp(0, __builtin_bit_cast(int, x), 0x124, 0xf, 0xf, false));
  x += __builtin_bit_cast(float, __builtin_amdgcn_update_dpp(0, __builtin_bit_cast(int, x), 0x122, 0xf, 0xf, false));
  x += __builtin_bit_cast(float, __builtin_amdgcn_update_dpp(0, __builtin_bit_cast(int, x), 0x121, 0xf, 0xf, false));
  return x;
}

DI void hedgehog_softmax(const f32x4& p0, const f32x4& p1, bool inval, u32x4& pos, u32x4& neg) {
  f32x2 v[4];
  v[0] = unpack2(pack2v(lo2(p0))); v[1] = unpack2(pack2v(hi2(p0)));
  v[2] = unpack2(pack2v(lo2(p1))); v[3] = unpack2(pack2v(hi2(p1)));
  f32x2 w2 = inval ? v[0] : v[2], w3 = inval ? v[1] : v[3];
  float mx = __builtin_fmaxf(__builtin_fmaxf(__builtin_fmaxf(v[0][0], v[0][1]), __builtin_fmaxf(v[1][0], v[1][1])),
                             __builtin_fmaxf(__builtin_fmaxf(w2[0], w2[1]), __builtin_fmaxf(w3[0], w3[1])));
  float mn = __builtin_fminf(__builtin_fminf(__builtin_fminf(v[0][0], v[0][1]), __builtin_fminf(v[1][0], v[1][1])),
                             __builtin_fminf(__builtin_fminf(w2[0], w2[1]), __builtin_fminf(w3[0], w3[1])));
  GRP_RED(mx, amax);
  GRP_RED(mn, amin);
  const float L = 1.4426950408889634f;
  const f32x2 L2 = {L, L}, nL2 = {-L, -L};
  const f32x2 bx = {-mx * L, -mx * L}, bn = {mn * L, mn * L};
  f32x2 e1[4], e2[4];
#pragma unroll
  for (int i = 0; i < 4; ++i) {
    f32x2 a = pk_fma(v[i], L2, bx), b = pk_fma(v[i], nL2, bn);
    e1[i] = f32x2{__builtin_amdgcn_exp2f(a[0]), __builtin_amdgcn_exp2f(a[1])};
    e2[i] = f32x2{__builtin_amdgcn_exp2f(b[0]), __builtin_amdgcn_exp2f(b[1])};
  }
  const f32x2 zz = {0.f, 0.f};
  e1[2] = inval ? zz : e1[2]; e1[3] = inval ? zz : e1[3];
  e2[2] = inval ? zz : e2[2]; e2[3] = inval ? zz : e2[3];
  f32x2 t1 = (e1[0] + e1[1]) + (e1[2] + e1[3]), t2 = (e2[0] + e2[1]) + (e2[2] + e2[3]);
  float s1 = t1[0] + t1[1], s2 = t2[0] + t2[1];
  GRP_RED(s1, aadd);
  GRP_RED(s2, aadd);
  float r1 = frcp(s1), r2 = frcp(s2);
  const f32x2 R1 = {r1, r1}, R2 = {r2, r2};
#pragma unroll
  for (int i = 0; i < 4; ++i) { pos[i] = pack2v(e1[i] * R1); neg[i] = pack2v(e2[i] * R2); }
}

template <int TYPE>
DI void k1_body(const __bf16* __restrict__ x, const __bf16* __restrict__ W, const __bf16* __restrict__ wphi,
                char* __restrict__ ws, lds_char* lds, int rb, int cg) {
  constexpr int NT = (TYPE == T_Q || TYPE == T_KV) ? 8 : 7;
  constexpr int LDW = (TYPE == T_Q || TYPE == T_KV) ? 1344 * 2 : 1792 * 2;
  const int tid = threadIdx.x, lane = tid & 63;
  const int wave = __builtin_amdgcn_readfirstlane(tid >> 6);
  const int wm = wave & 3, wn = wave >> 2;
  const int g = lane >> 4, l15 = lane & 15, q4 = (lane >> 2) & 3, p4 = lane & 3;
  const int row0 = rb * 128;
  const unsigned ldsb = (unsigned)(size_t)lds;

  unsigned goff[4], xoff[2];
#pragma unroll
  for (int i = 0; i < 4; ++i) {
    int row = (i * 8 + wave) * 2 + (lane >> 5);
    int sw = (row & 3) | (((row >> 3) & 1) << 2);
    int c = (lane & 31) ^ (sw << 1);
    int colb;
    if (TYPE == T_Q) { colb = cg * 448 + (c < 29 ? c : 0) * 16; }
    else if (TYPE == T_KV) {
      int j = cg - 2;
      colb = (c < 14) ? (896 + 224 * j + c * 16) : (c < 28 ? (1792 + 224 * j + (c - 14) * 16) : (896 + 224 * j));
    } else { colb = (cg - 6) * 448 + (c < 28 ? c : 0) * 16; }
    goff[i] = row * LDW + colb;
  }
#pragma unroll
  for (int i = 0; i < 2; ++i) {
    int row = (i * 8 + wave) * 8 + (lane >> 3);
    int c = (lane & 7) ^ ((row >> 1) & 7);
    xoff[i] = (row0 + row) * 896 + c * 16;
  }
  auto stage = [&](int chunk) {
    const unsigned dx = ldsb + K1_XOFF + (chunk % 3) * K1_XST + wave * 1024;
    const unsigned dw = ldsb + (chunk % 3) * K1_WST + wave * 1024;
    const char* xb = (const char*)x + chunk * 128;
    const char* wb = (const char*)W + (size_t)chunk * 64 * LDW;
    glds16(xb, xoff[0], dx);
    glds16(xb, xoff[1], dx + 8192);
#pragma unroll
    for (int i = 0; i < 4; ++i) glds16(wb, goff[i], dw + i * 8192);
  };
  stage(0);
  stage(1);
  stage(2);

  int baddr[NT];
#pragma unroll
  for (int nt = 0; nt < NT; ++nt) {
    int n0;
    if (TYPE == T_Q) n0 = 28 * (4 * wn + (nt >> 1)) + 16 * (nt & 1);
    else if (TYPE == T_KV) n0 = (nt < 4 ? 0 : 112) + 28 * (2 * wn + ((nt & 3) >> 1)) + 16 * (nt & 1);
    else n0 = 112 * wn + 16 * nt;
    int byte = 2 * (n0 + 4 * p4);
    int sw = q4 | ((g & 1) << 2);
    baddr[nt] = (8 * g + q4) * 512 + (((byte >> 4) ^ (sw << 1)) << 4) + (byte & 15);
  }
  int caddr0 = baddr[0], caddr1 = baddr[1];
#pragma unroll
  for (int w = 1; w < 4; ++w) {
    caddr0 = (wm == w) ? baddr[2 * w] : caddr0;
    caddr1 = (wm == w) ? baddr[(2 * w + 1 < NT) ? 2 * w + 1 : 2 * w] : caddr1;
  }
  const int xrow = wm * 32 + l15;
  const int xa0 = K1_XOFF + xrow * 128 + ((g ^ ((xrow >> 1) & 7)) << 4), xa1 = xa0 ^ 64;
  const int xs0 = xa0 + wn * 2048, xs1 = xa1 + wn * 2048;
  int baddrH[NT], caddr0H = caddr0 + 65536, caddr1H = caddr1 + 65536;
#pragma unroll
  for (int nt = 0; nt < NT; ++nt) { baddrH[nt] = baddr[nt] + 65536; asm volatile("" : "+v"(baddrH[nt])); }
  asm volatile("" : "+v"(caddr0H), "+v"(caddr1H));

  f32x4 acc[2][NT];
#pragma unroll
  for (int mt = 0; mt < 2; ++mt)
#pragma unroll
    for (int nt = 0; nt < NT; ++nt) acc[mt][nt] = f32x4{0.f, 0.f, 0.f, 0.f};
  f32x4 rs_ = {0.f, 0.f, 0.f, 0.f}, rq_ = rs_, cs0 = rs_, cs1 = rs_;
  const u32x4 ONES = {0x3f803f80u, 0x3f803f80u, 0x3f803f80u, 0x3f803f80u};
  STAMP(0, 0);

  struct Frags { u32x4 x0, x1, xs, c0, c1, b[NT]; };
  Frags fr[2];
  auto step = [&](const Frags& f, Frags& n, bool ld, int cn, int ksn, int sc) {
    const int st = cn % 3;
    const int wo = (st == 2 ? 0 : st * K1_WST) + ksn * 16384;
    const int xo = st * K1_XST;
    const unsigned dx = ldsb + K1_XOFF + ((sc < 0 ? 0 : sc) % 3) * K1_XST + wave * 1024;
    const unsigned dw = ldsb + ((sc < 0 ? 0 : sc) % 3) * K1_WST + wave * 1024;
    const char* xb = (const char*)x + sc * 128;
    const char* wb = (const char*)W + (size_t)(sc < 0 ? 0 : sc) * 64 * LDW;
#pragma unroll
    for (int nt = 0; nt < NT; ++nt) {
      if (ld) {
        if (nt == 0) { n.x0 = *(lds_u4*)(lds + (ksn ? xa1 : xa0) + xo); n.x1 = *(lds_u4*)(lds + (ksn ? xa1 : xa0) + xo + 2048); }
        lds_char* bp = lds + (st == 2 ? baddrH[nt] : baddr[nt]) + wo;
        n.b[nt] = tr_read2(bp, bp + 2048);
        if (nt == 3) { lds_char* cp = lds + (st == 2 ? caddr0H : caddr0) + wo; n.c0 = tr_read2(cp, cp + 2048); }
        if (nt == 4) { lds_char* cp = lds + (st == 2 ? caddr1H : caddr1) + wo; n.c1 = tr_read2(cp, cp + 2048); }
        if (nt == 5) n.xs = *(lds_u4*)(lds + (ksn ? xs1 : xs0) + xo);
      }
      const bool trans = (TYPE == T_Q) || (TYPE == T_Z1) || (TYPE == T_KV && nt < 4);
      acc[0][nt] = trans ? mfma16(f.b[nt], f.x0, acc[0][nt]) : mfma16(f.x0, f.b[nt], acc[0][nt]);
      acc[1][nt] = trans ? mfma16(f.b[nt], f.x1, acc[1][nt]) : mfma16(f.x1, f.b[nt], acc[1][nt]);
      if (nt == 1) cs0 = mfma16(ONES, f.c0, cs0);
      if (nt == 2) cs1 = mfma16(ONES, f.c1, cs1);
      if (nt == 3) rs_ = mfma16(ONES, f.xs, rs_);
      if (nt == 4) rq_ = mfma16(f.xs, f.xs, rq_);
      if (sc >= 0 && nt < 6) {
        if (nt < 2) glds16(xb, xoff[nt], dx + nt * 8192);
        else glds16(wb, goff[nt - 2], dw + (nt - 2) * 8192);
      }
      __builtin_amdgcn_sched_barrier(0);
    }
  };
  WAIT_VM(12);
  BARRIER();
  STAMP(0, 1);
  {
    Frags& n = fr[0];
    n.x0 = *(lds_u4*)(lds + xa0); n.x1 = *(lds_u4*)(lds + xa0 + 2048);
#pragma unroll
    for (int nt = 0; nt < NT; ++nt) n.b[nt] = tr_read2(lds + baddr[nt], lds + baddr[nt] + 2048);
    n.c0 = tr_read2(lds + caddr0, lds + caddr0 + 2048);
    n.c1 = tr_read2(lds + caddr1, lds + caddr1 + 2048);
    n.xs = *(lds_u4*)(lds + xs0);
  }
#pragma unroll
  for (int c = 0; c < 7; ++c) {
    step(fr[0], fr[1], true, c, 1, -1);
    if (c < 6) {
      if (c < 5) WAIT_VM(6); else WAIT_VM(0);
      WAIT_LGKM0();
      BARRIER();
      STAMP(0, 2 + c);
      if (c == 5 && (TYPE == T_Q || TYPE == T_KV)) {
        const int h0 = (TYPE == T_Q) ? cg * 8 : (cg - 2) * 4;
        unsigned o0 = min(h0 * 1568 + wave * 1024 + lane * 16, 25088 - 16);
        glds16(wphi, o0, ldsb + K1_WPHI + wave * 1024);
        const int w2 = 8 + (wave < 5 ? wave : 4);
        unsigned o1 = min(h0 * 1568 + w2 * 1024 + lane * 16, 25088 - 16);
        glds16(wphi, o1, ldsb + K1_WPHI + w2 * 1024);
      }
    }
    step(fr[1], fr[0], c < 6, c + 1, 0, (c + 3 < 7) ? c + 3 : -1);
  }
  STAMP(0, 8);

  lds_f* cstab = (lds_f*)(lds + K1_CS);
  lds_f* rtab = (lds_f*)(lds + K1_RT);
  lds_f* mtab = (lds_f*)(lds + K1_MT);
  {
    float s = rs_[0], c0 = cs0[0], c1 = cs1[0];
    float q = (p4 == 0) ? rq_[0] : (p4 == 1) ? rq_[1] : (p4 == 2) ? rq_[2] : rq_[3];
    q = (g == q4) ? q : 0.f;
    GRP_RED(q, aadd);
    float mean = s * (1.f / 448.f);
    float var = fmaxf(q * (1.f / 448.f) - mean * mean, 0.f);
    float mb = rbf(mean), vb = rbf(var);
    float sd = rbf(__builtin_amdgcn_sqrtf(rbf(vb + 1e-5f)));
    float r = frcp(sd);
    if (g == 0) {
      cstab[(wn * 8 + 2 * wm) * 16 + l15] = c0;
      cstab[(wn * 8 + 2 * wm + 1) * 16 + l15] = c1;
      rtab[wm * 32 + wn * 16 + l15] = r;
      mtab[wm * 32 + wn * 16 + l15] = -mb * r;
    }
  }
  WAIT_VM(0);
  STAMP(0, 9);
  __syncthreads();
  STAMP(0, 10);

  if (TYPE == T_Q || TYPE == T_KV || TYPE == T_Z1) {
    f32x2 Rr[2], Mr[2];
#pragma unroll
    for (int mt = 0; mt < 2; ++mt) {
      float r = rtab[wm * 32 + mt * 16 + l15], m = mtab[wm * 32 + mt * 16 + l15];
      Rr[mt] = f32x2{r, r}; Mr[mt] = f32x2{m, m};
    }
    if (TYPE == T_Z1) {
#pragma unroll
      for (int nt = 0; nt < NT; ++nt) {
        f32x4 c4 = *(lds_f4*)(cstab + (wn * 8 + nt) * 16 + 4 * g);
        const int f = (cg - 6) * 224 + wn * 112 + nt * 16 + 4 * g;
        const int R = (f & 31) >> 3, hh = (f >> 2) & 1;
#pragma unroll
        for (int mt = 0; mt < 2; ++mt) {
          f32x2 a = gelu2(pk_fma(lo2(c4), Mr[mt], lo2(acc[mt][nt]) * Rr[mt]));
          f32x2 b = gelu2(pk_fma(hi2(c4), Mr[mt], hi2(acc[mt][nt]) * Rr[mt]));
          const int s = row0 + wm * 32 + mt * 16 + l15;
          char* dst = ws + WS_Z1B + (size_t)((s >> 5) * 28 + (f >> 5)) * 2048 + R * 512 + ((s & 31) + 32 * hh) * 8;
          *(u32x2*)dst = u32x2{pack2v(a), pack2v(b)};
        }
      }
    } else {
      constexpr int NHW = (TYPE == T_Q) ? 4 : 2;
      const bool inval = (g == 3);
#pragma unroll
      for (int hh = 0; hh < NHW; ++hh) {
        const int hl = (TYPE == T_Q) ? (4 * wn + hh) : (2 * wn + hh);
        const int head = (TYPE == T_Q) ? (8 * cg + hl) : (4 * (cg - 2) + hl);
        lds_char* wp = lds + K1_WPHI + hl * 1568 + (4 * g + q4) * 56 + 8 * p4;
        u32x4 a0 = tr_read2(wp, wp + 16 * 56);
        u32x4 a1 = tr_read2(wp + 32, wp + 16 * 56 + 32);
        f32x4 c0 = *(lds_f4*)(cstab + (wn * 8 + 2 * hh) * 16 + 4 * g);
        f32x4 c1 = *(lds_f4*)(cstab + (wn * 8 + 2 * hh + 1) * 16 + 4 * g);
        lds_char* scr = lds + wave * 16384;
#pragma unroll
        for (int mt = 0; mt < 2; ++mt) {
          u32x4 bq;
          bq[0] = pack2v(pk_fma(lo2(c0), Mr[mt], lo2(acc[mt][2 * hh]) * Rr[mt]));
          bq[1] = pack2v(pk_fma(hi2(c0), Mr[mt], hi2(acc[mt][2 * hh]) * Rr[mt]));
          bq[2] = pack2v(pk_fma(lo2(c1), Mr[mt], lo2(acc[mt][2 * hh + 1]) * Rr[mt]));
          bq[3] = pack2v(pk_fma(hi2(c1), Mr[mt], hi2(acc[mt][2 * hh + 1]) * Rr[mt]));
          bq[2] = inval ? 0u : bq[2]; bq[3] = inval ? 0u : bq[3];
          f32x4 z4 = {0.f, 0.f, 0.f, 0.f};
          f32x4 p0 = mfma16(a0, bq, z4), p1 = mfma16(a1, bq, z4);
          u32x4 pos, neg;
          hedgehog_softmax(p0, p1, inval, pos, neg);
          if (TYPE == T_Q) {
            const int stile = rb * 8 + wm * 2 + mt;
            char* dst = ws + WS_PHIQ + ((size_t)(head * 128 + stile) * 2) * 1024 + lane * 16;
            *(u32x4*)dst = pos;
            *(u32x4*)(dst + 1024) = neg;
          } else {
            lds_char* d = scr + (mt * 16 + l15) * 160 + 8 * g;
            *(lds_u2*)(d) = u32x2{pos[0], pos[1]};
            *(lds_u2*)(d + 32) = u32x2{pos[2], pos[3]};
            *(lds_u2*)(d + 64) = u32x2{neg[0], neg[1]};
            *(lds_u2*)(d + 96) = u32x2{neg[2], neg[3]};
          }
        }
        if (TYPE == T_KV) {
          u32x4 vb[2];
#pragma unroll
          for (int dt = 0; dt < 2; ++dt) {
            const int nt = 4 + 2 * hh + dt;
            float csd = cstab[(wn * 8 + nt) * 16 + l15];
            const f32x2 cc = {csd, csd};
#pragma unroll
            for (int mt = 0; mt < 2; ++mt) {
              f32x4 r4 = *(lds_f4*)(rtab + wm * 32 + mt * 16 + 4 * g);
              f32x4 m4 = *(lds_f4*)(mtab + wm * 32 + mt * 16 + 4 * g);
              vb[dt][2 * mt] = pack2v(pk_fma(cc, lo2(m4), lo2(acc[mt][nt]) * lo2(r4)));
              vb[dt][2 * mt + 1] = pack2v(pk_fma(cc, hi2(m4), hi2(acc[mt][nt]) * hi2(r4)));
            }
            if (dt == 1) {
              const unsigned fill = (l15 == 12) ? 0x3f803f80u : 0u;
#pragma unroll
              for (int i = 0; i < 4; ++i) vb[1][i] = (l15 >= 12) ? fill : vb[1][i];
            }
          }
          lds_char* rp = scr + (4 * g + q4) * 160 + 8 * p4;
          f32x4 kv[4][2];
#pragma unroll
          for (int ft = 0; ft < 4; ++ft) {
            u32x4 pa = tr_read2(rp + ft * 32, rp + 16 * 160 + ft * 32);
            f32x4 z4 = {0.f, 0.f, 0.f, 0.f};
            kv[ft][0] = mfma16(pa, vb[0], z4);
            kv[ft][1] = mfma16(pa, vb[1], z4);
          }
          lds_char* q = lds + wave * 16384 + (hh == 0 ? 8192 : 0) + lane * 16;
          if (hh == 1) WAIT_LGKM0();
#pragma unroll
          for (int ft = 0; ft < 4; ++ft) {
            *(lds_f4*)(q + (ft * 2 + 0) * 1024) = kv[ft][0];
            *(lds_f4*)(q + (ft * 2 + 1) * 1024) = kv[ft][1];
          }
        }
      }
      if (TYPE == T_KV) {
        __syncthreads();
#pragma unroll
        for (int it = 0; it < 4; ++it) {
          int e = it * 512 + tid;
          int wn2 = e >> 10, pos = e & 1023;
          f32x4 a = *(lds_f4*)(lds + (wn2 * 4 + 0) * 16384 + pos * 16);
          a += *(lds_f4*)(lds + (wn2 * 4 + 1) * 16384 + pos * 16);
          a += *(lds_f4*)(lds + (wn2 * 4 + 2) * 16384 + pos * 16);
          a += *(lds_f4*)(lds + (wn2 * 4 + 3) * 16384 + pos * 16);
          int hh = 1 - (pos >> 9);
          int head = 4 * (cg - 2) + 2 * wn2 + hh;
          *(f32x4*)(ws + WS_KVS + ((size_t)(head * 16 + rb) * 8) * 1024 + (pos & 511) * 16) = a;
        }
      }
    }
  } else {
    lds_f* st = (lds_f*)(lds + K1_ST2);
#pragma unroll
    for (int mt = 0; mt < 2; ++mt) {
      f32x4 r4 = *(lds_f4*)(rtab + wm * 32 + mt * 16 + 4 * g);
      f32x4 m4 = *(lds_f4*)(mtab + wm * 32 + mt * 16 + 4 * g);
      f32x2 sa = {0.f, 0.f}, sb = {0.f, 0.f}, qa = {0.f, 0.f}, qb = {0.f, 0.f};
#pragma unroll
      for (int nt = 0; nt < NT; ++nt) {
        float csd = cstab[(wn * 8 + nt) * 16 + l15];
        const f32x2 cc = {csd, csd};
        f32x2 a = gelu2(pk_fma(cc, lo2(m4), lo2(acc[mt][nt]) * lo2(r4)));
        f32x2 b = gelu2(pk_fma(cc, hi2(m4), hi2(acc[mt][nt]) * hi2(r4)));
        unsigned pa = pack2v(a), pb = pack2v(b);
        a = unpack2(pa); b = unpack2(pb);
        sa += a; sb += b; qa = pk_fma(a, a, qa); qb = pk_fma(b, b, qb);
        const int f = (cg - 10) * 224 + wn * 112 + nt * 16 + l15;
        const int s16 = rb * 8 + wm * 2 + mt;
        char* dst = ws + WS_Z2B + (size_t)((f >> 5) * 128 + s16) * 1024 + ((f & 31) + 32 * (g >> 1)) * 16 + (g & 1) * 8;
        *(u32x2*)dst = u32x2{pa, pb};
      }
      f32x4 o0 = {row_sum16(sa[0]), row_sum16(qa[0]), row_sum16(sa[1]), row_sum16(qa[1])};
      f32x4 o1 = {row_sum16(sb[0]), row_sum16(qb[0]), row_sum16(sb[1]), row_sum16(qb[1])};
      if (l15 == 0) {
        *(lds_f4*)(st + (wn * 128 + wm * 32 + mt * 16 + 4 * g) * 2) = o0;
        *(lds_f4*)(st + (wn * 128 + wm * 32 + mt * 16 + 4 * g) * 2 + 4) = o1;
      }
    }
    __syncthreads();
    if (tid < 128) {
      f32x2 a = *(lds_f2*)(st + tid * 2), b = *(lds_f2*)(st + (128 + tid) * 2);
      *(f32x2*)(ws + WS_STAT + ((size_t)(cg - 10) * 2048 + row0 + tid) * 8) = a + b;
    }
  }
}

__global__ __launch_bounds__(512, 2) void k1_kernel(const __bf16* __restrict__ x, const __bf16* __restrict__ wqkv,
                                                    const __bf16* __restrict__ wphi, const __bf16* __restrict__ win,
                                                    char* __restrict__ ws) {
  extern __shared__ __attribute__((aligned(16))) char smem[];
  lds_char* lds = (lds_char*)smem;
  const int b = blockIdx.x;
  const int xcd = b & 7, idx = b >> 3;
  const int rb = 2 * xcd + idx / 14, cg = idx % 14;
  if (cg < 2) k1_body<T_Q>(x, wqkv, wphi, ws, lds, rb, cg);
  else if (cg < 6) k1_body<T_KV>(x, wqkv, wphi, ws, lds, rb, cg);
  else if (cg < 10) k1_body<T_Z1>(x, win, wphi, ws, lds, rb, cg);
  else k1_body<T_Z2>(x, win, wphi, ws, lds, rb, cg);
  STAMP(0, 11);
}

constexpr int K2_WB = 0;
constexpr int K2_RT = 32768;
constexpr int K2_MT = K2_RT + 8192;
constexpr int K2_CT = K2_MT + 4096;
constexpr int K2_LDS = K2_CT + 256;

__global__ __launch_bounds__(512, 2) void k2_kernel(const __bf16* __restrict__ wsp, const __bf16* __restrict__ bsp,
                                                    char* __restrict__ ws) {
  extern __shared__ __attribute__((aligned(16))) char smem[];
  lds_char* lds = (lds_char*)smem;
  const int tid = threadIdx.x, lane = tid & 63;
  const int wave = __builtin_amdgcn_readfirstlane(tid >> 6);
  const int b = blockIdx.x, xcd = b & 7, idx = b >> 3;
  STAMP(1, 0);
  if (idx >= 28) {
    const int e = xcd * 4 + (idx - 28), head = e >> 1, half = e & 1;
    const int g = lane >> 4, l15 = lane & 15;
    u32x4 pq[8][2];
    const char* pbase = ws + WS_PHIQ + ((size_t)(head * 128 + half * 64 + wave * 8) * 2) * 1024 + lane * 16;
#pragma unroll
    for (int i = 0; i < 8; ++i) { pq[i][0] = *(const u32x4*)(pbase + i * 2048); pq[i][1] = *(const u32x4*)(pbase + i * 2048 + 1024); }
    {
      const char* kb = ws + WS_KVS + ((size_t)(head * 16) * 8 + wave) * 1024 + lane * 16;
      f32x4 a = {0.f, 0.f, 0.f, 0.f};
#pragma unroll
      for (int r = 0; r < 16; ++r) a += *(const f32x4*)(kb + (size_t)r * 8192);
      *(lds_u2*)(lds + wave * 512 + lane * 8) = u32x2{pack2(a[0], a[1]), pack2(a[2], a[3])};
    }
    __syncthreads();
    u32x4 af[2][2];
#pragma unroll
    for (int sg = 0; sg < 2; ++sg)
#pragma unroll
      for (int dt = 0; dt < 2; ++dt) {
        u32x2 lo = *(lds_u2*)(lds + ((2 * sg) * 2 + dt) * 512 + lane * 8);
        u32x2 hi = *(lds_u2*)(lds + ((2 * sg + 1) * 2 + dt) * 512 + lane * 8);
        af[sg][dt] = u32x4{lo[0], lo[1], hi[0], hi[1]};
      }
#pragma unroll
    for (int i = 0; i < 8; ++i) {
      f32x4 z4 = {0.f, 0.f, 0.f, 0.f};
      f32x4 n0 = mfma16(af[0][0], pq[i][0], z4); n0 = mfma16(af[1][0], pq[i][1], n0);
      f32x4 n1 = mfma16(af[0][1], pq[i][0], z4); n1 = mfma16(af[1][1], pq[i][1], n1);
      float den = __shfl(n1[0], 48 + l15);
      den = rbf(rbf(den) + 1e-6f);
      float rd = 1.f / den;
      const int s = half * 1024 + (wave * 8 + i) * 16 + l15;
#pragma unroll
      for (int dt = 0; dt < 2; ++dt) {
        const f32x4& n = dt ? n1 : n0;
        u32x2 o = {pack2(rbf(n[0]) * rd, rbf(n[1]) * rd), pack2(rbf(n[2]) * rd, rbf(n[3]) * rd)};
        const int k = 28 * head + 16 * dt + 4 * g;
        const int kl = k & 15, hh = (kl >> 2) & 1, j0 = (kl >> 3) * 4;
        char* dst = ws + WS_HO + (size_t)((s >> 5) * 84 + (k >> 4)) * 1024 + ((s & 31) + 32 * hh) * 16 + j0 * 2;
        if (!(dt == 1 && g == 3)) *(u32x2*)dst = o;
      }
    }
    return;
  }

  const int rb = 4 * xcd + idx / 7, ct = idx % 7;
  constexpr int NK = 16;
  const int koff = (ct * 2 + (ct > 3 ? 1 : 0)) & 15;
#define KROT(k) ((((k) + koff) & 15))
  f32x2 sa[4][4];
  auto stat_loads = [&]() {
#pragma unroll
    for (int i = 0; i < 4; ++i)
#pragma unroll
      for (int gq = 0; gq < 4; ++gq) sa[i][gq] = *(const f32x2*)(ws + WS_STAT + ((size_t)gq * 2048 + i * 512 + tid) * 8);
  };
  auto stat_tables = [&]() {
    lds_f* rt = (lds_f*)(lds + K2_RT);
    __attribute__((address_space(3))) unsigned short* mt = (__attribute__((address_space(3))) unsigned short*)(lds + K2_MT);
#pragma unroll
    for (int i = 0; i < 4; ++i) {
      f32x2 t = (sa[i][0] + sa[i][1]) + (sa[i][2] + sa[i][3]);
      float mean = t[0] * (1.f / 896.f);
      float var = fmaxf(t[1] * (1.f / 896.f) - mean * mean, 0.f);
      float vb = rbf(var);
      float sd = rbf(__builtin_amdgcn_sqrtf(rbf(vb + 1e-5f)));
      rt[i * 512 + tid] = frcp(sd);
      mt[i * 512 + tid] = (unsigned short)pack2(mean, 0.f);
    }
  };
  if (wave >= 4) {
    __builtin_amdgcn_s_setprio(2);
    const int pt = tid - 256, t = pt >> 2, cp = pt & 3;
    const int g = lane >> 4, l15 = lane & 15;
    const char* src = (const char*)wsp + (size_t)(rb * 64 + t) * 4096 + cp * 16;
    constexpr int PD = 4;
    u32x4 wr[PD][4];
#pragma unroll
    for (int j = 0; j < 4; ++j) wr[0][j] = *(const u32x4*)(src + KROT(0) * 256 + j * 64);
    stat_loads();
    __builtin_amdgcn_sched_barrier(0);
    stat_tables();
    __builtin_amdgcn_sched_barrier(0);
#pragma unroll
    for (int i = 1; i < PD; ++i)
#pragma unroll
      for (int j = 0; j < 4; ++j) wr[i][j] = *(const u32x4*)(src + KROT(i) * 256 + j * 64);
    __builtin_amdgcn_sched_barrier(0);
    int wa[4];
#pragma unroll
    for (int j = 0; j < 4; ++j) wa[j] = t * 256 + (((4 * j + cp) ^ (t & 15)) << 4);
    const int ta = cp * 32;
    const int crow = (wave - 4) * 16 + l15;
    const int ca = crow * 256 + ((g ^ l15) << 4);
    f32x4 cacc = {0.f, 0.f, 0.f, 0.f};
    WAIT_LGKM0(); BARRIER();
    f32x4 rr[2][4][2];
#pragma unroll
    for (int j = 0; j < 4; ++j) {
      const int so = (KROT(0) * 128 + j * 32) * 4;
      rr[0][j][0] = *(lds_f4*)(lds + K2_RT + so + ta); rr[0][j][1] = *(lds_f4*)(lds + K2_RT + so + ta + 16);
    }
#pragma unroll
    for (int k = 0; k <= NK; ++k) {
      lds_char* wb = lds + K2_WB + (k & 1) * 16384;
      lds_char* pb = lds + K2_WB + ((k + 1) & 1) * 16384;
      u32x4 mf[4], wf[4];
#pragma unroll
      for (int j = 0; j < 4; ++j) {
        if (k > 0) {
          mf[j] = *(lds_u4*)(lds + K2_MT + (KROT(k - 1) * 128 + j * 32 + g * 8) * 2);
          wf[j] = *(lds_u4*)(pb + (ca ^ (j << 6)));
        }
        if (k + 1 < NK) {
          const int so = (KROT(k + 1) * 128 + j * 32) * 4;
          rr[(k + 1) & 1][j][0] = *(lds_f4*)(lds + K2_RT + so + ta); rr[(k + 1) & 1][j][1] = *(lds_f4*)(lds + K2_RT + so + ta + 16);
        }
      }
      __builtin_amdgcn_sched_barrier(0);
#pragma unroll
      for (int j = 0; j < 4; ++j) {
        if (k < NK) {
          const f32x4 r0 = rr[k & 1][j][0], r1 = rr[k & 1][j][1];
          u32x4 w = wr[k % PD][j];
          if (k + PD < NK) wr[k % PD][j] = *(const u32x4*)(src + KROT(k + PD) * 256 + j * 64);
          u32x4 o;
          o[0] = pack2(smul(bf_lo(w[0]), r0[0]), smul(bf_hi(w[0]), r0[1]));
          o[1] = pack2(smul(bf_lo(w[1]), r0[2]), smul(bf_hi(w[1]), r0[3]));
          o[2] = pack2(smul(bf_lo(w[2]), r1[0]), smul(bf_hi(w[2]), r1[1]));
          o[3] = pack2(smul(bf_lo(w[3]), r1[2]), smul(bf_hi(w[3]), r1[3]));
          *(lds_u4*)(wb + wa[j]) = o;
        }
        __builtin_amdgcn_sched_barrier(0);
        if (k > 0) cacc = mfma16(mf[j], wf[j], cacc);
      }
      if (k == NK) {
        if (g == 0) *(lds_f*)(lds + K2_CT + crow * 4) = cacc[0];
      }
      WAIT_LGKM0();
      BARRIER();
    }
  } else {
    const int F = 4 * ct + wave, l31 = lane & 31, hh = lane >> 5;
    const char* zsrc = ws + WS_Z2B + (size_t)F * 128 * 1024 + lane * 16;
    constexpr int ZD = 24;
    u32x4 zf[ZD];
#pragma unroll
    for (int i = 0; i < 8; ++i) zf[i] = *(const u32x4*)(zsrc + (KROT(i >> 3) * 8 + (i & 7)) * 1024);
    stat_loads();
    __builtin_amdgcn_sched_barrier(0);
    stat_tables();
    __builtin_amdgcn_sched_barrier(0);
#pragma unroll
    for (int i = 8; i < ZD; ++i) zf[i] = *(const u32x4*)(zsrc + (KROT(i >> 3) * 8 + (i & 7)) * 1024);
    __builtin_amdgcn_sched_barrier(0);
    u32x2 z1v[2][4];
    float bb[2];
#pragma unroll
    for (int tt = 0; tt < 2; ++tt) {
      const int t = rb * 64 + tt * 32 + l31;
      bb[tt] = bf_lo((unsigned)*(const unsigned short*)(bsp + t));
      const char* z1p = ws + WS_Z1B + (size_t)((t >> 5) * 28 + F) * 2048 + lane * 8;
#pragma unroll
      for (int R = 0; R < 4; ++R) z1v[tt][R] = *(const u32x2*)(z1p + R * 512);
    }
    const int ra = l31 * 256 + ((hh ^ (l31 & 15)) << 4);
    f32x16 acc[2];
#pragma unroll
    for (int i = 0; i < 16; ++i) { acc[0][i] = 0.f; acc[1][i] = 0.f; }
    WAIT_LGKM0(); BARRIER();
    STAMP(1, 1);
    BARRIER();
    STAMP(1, 2);
    int rk[8];
#pragma unroll
    for (int kk = 0; kk < 8; ++kk) { rk[kk] = ra ^ (kk << 5); }
    u32x4 w0 = *(lds_u4*)(lds + rk[0]), w1 = *(lds_u4*)(lds + rk[0] + 8192);
#pragma unroll
    for (int k = 0; k < NK; ++k) {
#pragma unroll
      for (int kk = 0; kk < 8; ++kk) {
        const int ks = k * 8 + kk;
        u32x4 n0 = w0, n1 = w1;
        if (kk == 7 && k + 1 < NK) { WAIT_LGKM0(); BARRIER(); }
        if (ks + 1 < 128) {
          const int nb = (((ks + 1) >> 3) & 1) * 16384;
          n0 = *(lds_u4*)(lds + rk[(kk + 1) & 7] + nb); n1 = *(lds_u4*)(lds + rk[(kk + 1) & 7] + nb + 8192);
        }
        u32x4 z = zf[ks % ZD];
        if (ks + ZD < 128) zf[ks % ZD] = *(const u32x4*)(zsrc + (KROT((ks + ZD) >> 3) * 8 + ((ks + ZD) & 7)) * 1024);
        acc[0] = mfma32(z, w0, acc[0]);
        acc[1] = mfma32(z, w1, acc[1]);
        w0 = n0; w1 = n1;
        __builtin_amdgcn_sched_barrier(0);
      }
    }
    WAIT_LGKM0();
    BARRIER();
    STAMP(1, 3);
#pragma unroll
    for (int tt = 0; tt < 2; ++tt) {
      const int t = rb * 64 + tt * 32 + l31;
      const float c = *(lds_f*)(lds + K2_CT + (tt * 32 + l31) * 4);
      const f32x2 cc = {c, c}, b2 = {bb[tt], bb[tt]};
      unsigned hp[8];
#pragma unroll
      for (int R = 0; R < 4; ++R) {
        f32x2 g0 = unpack2(pack2v(f32x2{acc[tt][4 * R + 0], acc[tt][4 * R + 1]} - cc));
        f32x2 g1 = unpack2(pack2v(f32x2{acc[tt][4 * R + 2], acc[tt][4 * R + 3]} - cc));
        g0 = unpack2(pack2v(g0 + b2)); g1 = unpack2(pack2v(g1 + b2));
        hp[2 * R] = pack2v(unpack2(z1v[tt][R][0]) * g0); hp[2 * R + 1] = pack2v(unpack2(z1v[tt][R][1]) * g1);
      }
      char* dst = ws + WS_HO + (size_t)((t >> 5) * 84 + 28 + 2 * F) * 1024 + lane * 16;
      *(u32x4*)dst = u32x4{hp[0], hp[1], hp[2], hp[3]};
      *(u32x4*)(dst + 1024) = u32x4{hp[4], hp[5], hp[6], hp[7]};
    }
    STAMP(1, 4);
  }
}

constexpr int K3_BUF = 8192;
constexpr int K3_NB = 4;
constexpr int K3_LDS = K3_BUF * K3_NB;

__global__ __launch_bounds__(256, 2) void k3_kernel(const __bf16* __restrict__ x, const __bf16* __restrict__ wo,
                                                    const __bf16* __restrict__ wout, const char* __restrict__ ws,
                                                    __bf16* __restrict__ out) {
  extern __shared__ __attribute__((aligned(16))) char smem[];
  lds_char* lds = (lds_char*)smem;
  const int tid = threadIdx.x, lane = tid & 63;
  const int wave = __builtin_amdgcn_readfirstlane(tid >> 6);
  const int b = blockIdx.x, xcd = b & 7, idx = b >> 3;
  const int rb = 4 * xcd + idx / 7, ct = idx % 7;
  const int wt = wave & 1, wn = wave >> 1;
  const int l31 = lane & 31, hh = lane >> 5, gi = lane >> 4, q4 = (lane >> 2) & 3, p4 = lane & 3;

  int goff[2];
#pragma unroll
  for (int i = 0; i < 2; ++i) {
    int row = (i * 4 + wave) * 8 + (lane >> 3);
    int sw = ((row & 3) << 1) ^ (((row >> 3) & 1));
    int c = (lane & 7) ^ sw;
    goff[i] = row * 896 + ct * 128 + c * 16;
  }
  auto stage = [&](int chunk) {
    const char* base = (chunk < 7) ? ((const char*)wo + (size_t)chunk * 64 * 896) : ((const char*)wout + (size_t)(chunk - 7) * 64 * 896);
    const int buf = chunk % K3_NB;
#pragma unroll
    for (int i = 0; i < 2; ++i)
      glds16(base, (unsigned)goff[i], (unsigned)(size_t)lds + buf * K3_BUF + (i * 4 + wave) * 1024);
  };
  stage(0); stage(1); stage(2);

  const int T32 = rb * 2 + wt;
  const char* asrc = ws + WS_HO + (size_t)T32 * 84 * 1024 + lane * 16;
  constexpr int AD = 12;
  u32x4 af[AD];
#pragma unroll
  for (int i = 0; i < AD; ++i) af[i] = *(const u32x4*)(asrc + i * 1024);

  int baddr;
  {
    int row = 4 * (gi >> 1) + q4;
    int byte = 2 * (wn * 32 + (gi & 1) * 16 + 4 * p4);
    int sw = ((row & 3) << 1);
    baddr = row * 128 + (((byte >> 4) ^ sw) << 4) + (byte & 15);
  }
  const int baddr2 = (baddr ^ 16) + 8 * 128;

  f32x16 acc;
#pragma unroll
  for (int i = 0; i < 16; ++i) acc[i] = 0.f;
  unsigned t1[8];
  const int t = rb * 64 + wt * 32 + l31;
  const __bf16* xr = x + (size_t)t * D_ + ct * 64 + wn * 32 + 4 * hh;
  u32x2 xv[4];
#pragma unroll
  for (int R = 0; R < 4; ++R) xv[R] = *(const u32x2*)(xr + 8 * R);

#pragma unroll
  for (int c = 0; c < 21; ++c) {
    {
      int younger = 0;
      if (c < 3) younger = (2 - c) * 2 + AD + 4;
      for (int j = (c < 3 ? 0 : c - 3); j < c; ++j) {
        if (j != c - 3 && j + 3 < 21) younger += 2;
        for (int kk = 0; kk < 4; ++kk) if (j * 4 + kk + AD < 84) younger += 1;
      }
      WAIT_VMI(younger);
    }
    WAIT_LGKM0();
    BARRIER();
    if (c + 3 < 21) stage(c + 3);
    lds_char* bufp = lds + (c % K3_NB) * K3_BUF;
#pragma unroll
    for (int kk = 0; kk < 4; ++kk) {
      const int ks = c * 4 + kk;
      u32x4 w = tr_read2(bufp + baddr + kk * 2048, bufp + baddr2 + kk * 2048);
      u32x4 a = af[ks % AD];
      if (ks + AD < 84) af[ks % AD] = *(const u32x4*)(asrc + (size_t)(ks + AD) * 1024);
      acc = mfma32(w, a, acc);
    }
    if (c == 6) {
#pragma unroll
      for (int R = 0; R < 4; ++R) {
        float v0 = rbf(bf_lo(xv[R][0]) + rbf(acc[4 * R + 0]));
        float v1 = rbf(bf_hi(xv[R][0]) + rbf(acc[4 * R + 1]));
        float v2 = rbf(bf_lo(xv[R][1]) + rbf(acc[4 * R + 2]));
        float v3 = rbf(bf_hi(xv[R][1]) + rbf(acc[4 * R + 3]));
        t1[2 * R] = pack2(v0, v1); t1[2 * R + 1] = pack2(v2, v3);
      }
#pragma unroll
      for (int i = 0; i < 16; ++i) acc[i] = 0.f;
    }
  }
  __bf16* orow = out + (size_t)t * D_ + ct * 64 + wn * 32 + 4 * hh;
#pragma unroll
  for (int R = 0; R < 4; ++R) {
    float v0 = bf_lo(t1[2 * R]) + rbf(acc[4 * R + 0]);
    float v1 = bf_hi(t1[2 * R]) + rbf(acc[4 * R + 1]);
    float v2 = bf_lo(t1[2 * R + 1]) + rbf(acc[4 * R + 2]);
    float v3 = bf_hi(t1[2 * R + 1]) + rbf(acc[4 * R + 3]);
    *(u32x2*)(orow + 8 * R) = u32x2{pack2(v0, v1), pack2(v2, v3)};
  }
}

extern "C" void kernel_launch(void* const* d_in, const int* in_sizes, int n_in, void* d_out, int out_size,
                              void* d_ws, size_t ws_size, hipStream_t stream) {
  const __bf16* x = (const __bf16*)d_in[0];
  const __bf16* wqkv = (const __bf16*)d_in[1];
  const __bf16* wphi = (const __bf16*)d_in[2];
  const __bf16* wo = (const __bf16*)d_in[3];
  const __bf16* win = (const __bf16*)d_in[5];
  const __bf16* wsp = (const __bf16*)d_in[6];
  const __bf16* bsp = (const __bf16*)d_in[7];
  const __bf16* wout = (const __bf16*)d_in[8];
  char* ws = (char*)d_ws;
  (void)hipFuncSetAttribute((const void*)k1_kernel, hipFuncAttributeMaxDynamicSharedMemorySize, K1_LDS);
  k1_kernel<<<224, 512, K1_LDS, stream>>>(x, wqkv, wphi, win, ws);
  k2_kernel<<<256, 512, K2_LDS, stream>>>(wsp, bsp, ws);
  k3_kernel<<<224, 256, K3_LDS, stream>>>(x, wo, wout, ws, (__bf16*)d_out);
}
```

```cpp
#include <hip/hip_runtime.h>
#include <hip/hip_bf16.h>
#include <stdint.h>

#define DI __device__ __forceinline__
typedef __attribute__((ext_vector_type(8))) __bf16 bf16x8;
typedef __attribute__((ext_vector_type(2))) __bf16 bf16x2;
typedef __attribute__((ext_vector_type(4))) short s16x4;
typedef __attribute__((ext_vector_type(4))) float f32x4;
typedef __attribute__((ext_vector_type(2))) float f32x2;
typedef __attribute__((ext_vector_type(16))) float f32x16;
typedef __attribute__((ext_vector_type(4))) unsigned u32x4;
typedef __attribute__((ext_vector_type(2))) unsigned u32x2;
typedef __attribute__((address_space(3))) char lds_char;
typedef __attribute__((address_space(3))) s16x4 lds_s16x4;

constexpr int S_ = 2048, D_ = 448;

constexpr size_t WS_PHIQ = 0;
constexpr size_t WS_KVS = 4u << 20;
constexpr size_t WS_Z1B = 6u << 20;
constexpr size_t WS_Z2B = 10u << 20;
constexpr size_t WS_STAT = 14u << 20;
constexpr size_t WS_HO = 15u << 20;

DI unsigned pack2(float a, float b) {
  f32x2 v = {a, b};
  return __builtin_bit_cast(unsigned, __builtin_convertvector(v, bf16x2));
}
DI float bf_lo(unsigned u) { return __uint_as_float(u << 16); }
DI float bf_hi(unsigned u) { return __uint_as_float(u & 0xffff0000u); }
DI float rbf(float x) { return bf_lo(pack2(x, 0.f)); }
DI float dot2(unsigned a, unsigned b, float c) {
  return __builtin_amdgcn_fdot2_f32_bf16(__builtin_bit_cast(bf16x2, a), __builtin_bit_cast(bf16x2, b), c, false);
}
DI f32x4 mfma16(u32x4 a, u32x4 b, f32x4 c) {
  return __builtin_amdgcn_mfma_f32_16x16x32_bf16(__builtin_bit_cast(bf16x8, a), __builtin_bit_cast(bf16x8, b), c, 0, 0, 0);
}
DI f32x16 mfma32(u32x4 a, u32x4 b, f32x16 c) {
  return __builtin_amdgcn_mfma_f32_32x32x16_bf16(__builtin_bit_cast(bf16x8, a), __builtin_bit_cast(bf16x8, b), c, 0, 0, 0);
}
DI u32x2 tr_read(lds_char* p) {
  s16x4 r = __builtin_amdgcn_ds_read_tr16_b64_v4i16((lds_s16x4*)p);
  return __builtin_bit_cast(u32x2, r);
}
DI u32x4 tr_read2(lds_char* p0, lds_char* p1) {
  u32x2 a = tr_read(p0), b = tr_read(p1);
  u32x4 r = {a[0], a[1], b[0], b[1]};
  return r;
}
DI float fexp(float x) { return __builtin_amdgcn_exp2f(x * 1.4426950408889634f); }
DI float frcp(float x) { return __builtin_amdgcn_rcpf(x); }
DI float gelu_tanh(float x) {
  float u = 0.7978845608028654f * (x + 0.044715f * x * x * x);
  float e = __builtin_amdgcn_exp2f(-2.885390081777927f * u);
  return x * frcp(1.f + e);
}
DI const char* uni_ptr(const char* p) {
  unsigned long long v = (unsigned long long)p;
  unsigned lo = __builtin_amdgcn_readfirstlane((unsigned)v), hi = __builtin_amdgcn_readfirstlane((unsigned)(v >> 32));
  return (const char*)(((unsigned long long)hi << 32) | lo);
}
DI void glds16(const void* sbase, unsigned voff, unsigned lds_dst) {
  asm volatile("s_mov_b32 m0, %2\n\ts_nop 0\n\tglobal_load_lds_dwordx4 %0, %1"
               :: "v"(voff), "s"(sbase), "s"(lds_dst) : "memory");
}
#define STAMP(kid, i)
#define RSTAMP(kid, i)
#define WAIT_VMI(n) asm volatile("s_waitcnt vmcnt(%0)" :: "i"(n) : "memory")
#define WAIT_VM(n) asm volatile("s_waitcnt vmcnt(" #n ")" ::: "memory")
#define WAIT_LGKM0() asm volatile("s_waitcnt lgkmcnt(0)" ::: "memory")
#define BARRIER() __builtin_amdgcn_s_barrier()

constexpr int K1_WST = 32768;
constexpr int K1_XOFF = 98304;
constexpr int K1_XST = 16384;
constexpr int K1_WPHI = 131072;
constexpr int K1_CS = K1_WPHI + 13312;
constexpr int K1_RT = K1_CS + 1024;
constexpr int K1_MT = K1_RT + 512;
constexpr int K1_ST2 = K1_MT + 512;
constexpr int K1_LDS = 147456 + 2048;

enum { T_Q = 0, T_KV = 1, T_Z1 = 2, T_Z2 = 3 };

typedef __attribute__((address_space(3))) float lds_f;
typedef __attribute__((address_space(3))) f32x4 lds_f4;
typedef __attribute__((address_space(3))) f32x2 lds_f2;
typedef __attribute__((address_space(3))) u32x4 lds_u4;
typedef __attribute__((address_space(3))) u32x2 lds_u2;

DI float amax(float a, float b) { float r; asm("v_max_f32 %0, %1, %2" : "=v"(r) : "v"(a), "v"(b)); return r; }
DI float amin(float a, float b) { float r; asm("v_min_f32 %0, %1, %2" : "=v"(r) : "v"(a), "v"(b)); return r; }
#define GRP_RED(x, OP) do { \
    auto r_ = __builtin_amdgcn_permlane16_swap(__float_as_uint(x), __float_as_uint(x), false, false); \
    x = OP(__uint_as_float(r_[0]), __uint_as_float(r_[1])); \
    auto q_ = __builtin_amdgcn_permlane32_swap(__float_as_uint(x), __float_as_uint(x), false, false); \
    x = OP(__uint_as_float(q_[0]), __uint_as_float(q_[1])); } while (0)
DI float aadd(float a, float b) { return a + b; }
DI float smul(float a, float b) { float r; asm("v_mul_f32 %0, %1, %2" : "=v"(r) : "v"(a), "v"(b)); return r; }
DI f32x2 pk_fma(f32x2 a, f32x2 b, f32x2 c) { return __builtin_elementwise_fma(a, b, c); }
DI f32x2 lo2(const f32x4& v) { return f32x2{v[0], v[1]}; }
DI f32x2 hi2(const f32x4& v) { return f32x2{v[2], v[3]}; }
DI unsigned pack2v(f32x2 v) { return __builtin_bit_cast(unsigned, __builtin_convertvector(v, bf16x2)); }
DI f32x2 unpack2(unsigned u) { return f32x2{bf_lo(u), bf_hi(u)}; }
DI f32x2 gelu2(f32x2 v) {
  const f32x2 c1 = {-2.302208198f, -2.302208198f};
  const f32x2 c2 = {-0.1029432397f, -0.1029432397f};
  f32x2 u = v * pk_fma(v * v, c2, c1);
  f32x2 d = f32x2{__builtin_amdgcn_exp2f(u[0]), __builtin_amdgcn_exp2f(u[1])} + f32x2{1.f, 1.f};
  return v * f32x2{frcp(d[0]), frcp(d[1])};
}
DI float row_sum16(float x) {
  x += __builtin_bit_cast(float, __builtin_amdgcn_update_dpp(0, __builtin_bit_cast(int, x), 0x128, 0xf, 0xf, false));
  x += __builtin_bit_cast(float, __builtin_amdgcn_update_dpp(0, __builtin_bit_cast(int, x), 0x124, 0xf, 0xf, false));
  x += __builtin_bit_cast(float, __builtin_amdgcn_update_dpp(0, __builtin_bit_cast(int, x), 0x122, 0xf, 0xf, false));
  x += __builtin_bit_cast(float, __builtin_amdgcn_update_dpp(0, __builtin_bit_cast(int, x), 0x121, 0xf, 0xf, false));
  return x;
}

DI void hedgehog_softmax(const f32x4& p0, const f32x4& p1, bool inval, u32x4& pos, u32x4& neg) {
  f32x2 v[4];
  v[0] = unpack2(pack2v(lo2(p0))); v[1] = unpack2(pack2v(hi2(p0)));
  v[2] = unpack2(pack2v(lo2(p1))); v[3] = unpack2(pack2v(hi2(p1)));
  f32x2 w2 = inval ? v[0] : v[2], w3 = inval ? v[1] : v[3];
  float mx = __builtin_fmaxf(__builtin_fmaxf(__builtin_fmaxf(v[0][0], v[0][1]), __builtin_fmaxf(v[1][0], v[1][1])),
                             __builtin_fmaxf(__builtin_fmaxf(w2[0], w2[1]), __builtin_fmaxf(w3[0], w3[1])));
  float mn = __builtin_fminf(__builtin_fminf(__builtin_fminf(v[0][0], v[0][1]), __builtin_fminf(v[1][0], v[1][1])),
                             __builtin_fminf(__builtin_fminf(w2[0], w2[1]), __builtin_fminf(w3[0], w3[1])));
  GRP_RED(mx, amax);
  GRP_RED(mn, amin);
  const float L = 1.4426950408889634f;
  const f32x2 L2 = {L, L}, nL2 = {-L, -L};
  const f32x2 bx = {-mx * L, -mx * L}, bn = {mn * L, mn * L};
  f32x2 e1[4], e2[4];
#pragma unroll
  for (int i = 0; i < 4; ++i) {
    f32x2 a = pk_fma(v[i], L2, bx), b = pk_fma(v[i], nL2, bn);
    e1[i] = f32x2{__builtin_amdgcn_exp2f(a[0]), __builtin_amdgcn_exp2f(a[1])};
    e2[i] = f32x2{__builtin_amdgcn_exp2f(b[0]), __builtin_amdgcn_exp2f(b[1])};
  }
  const f32x2 zz = {0.f, 0.f};
  e1[2] = inval ? zz : e1[2]; e1[3] = inval ? zz : e1[3];
  e2[2] = inval ? zz : e2[2]; e2[3] = inval ? zz : e2[3];
  f32x2 t1 = (e1[0] + e1[1]) + (e1[2] + e1[3]), t2 = (e2[0] + e2[1]) + (e2[2] + e2[3]);
  float s1 = t1[0] + t1[1], s2 = t2[0] + t2[1];
  GRP_RED(s1, aadd);
  GRP_RED(s2, aadd);
  float r1 = frcp(s1), r2 = frcp(s2);
  const f32x2 R1 = {r1, r1}, R2 = {r2, r2};
#pragma unroll
  for (int i = 0; i < 4; ++i) { pos[i] = pack2v(e1[i] * R1); neg[i] = pack2v(e2[i] * R2); }
}

DI void k1_half(int cg, int wn, int& ht, int& j) {
  if (cg < 8) { if (wn == 0) { ht = T_KV; j = cg; } else if (cg < 4) { ht = T_Q; j = cg; } else { ht = T_Z1; j = cg - 4; } }
  else if (cg < 10) { ht = T_Z1; j = 4 + 2 * (cg - 8) + wn; }
  else { ht = T_Z2; j = 2 * (cg - 10) + wn; }
}

template <int TYPE>
DI void k1_wave(const __bf16* __restrict__ x, const char* Wh0, const char* Wh1, int ldw0, int ldw1,
                const unsigned (&goff)[4], const unsigned (&xoff)[2], const __bf16* __restrict__ wphi,
                char* __restrict__ ws, lds_char* lds, int rb, int cg, int hj) {
  constexpr int NT = (TYPE == T_Q || TYPE == T_KV) ? 8 : 7;
  const int tid = threadIdx.x, lane = tid & 63;
  const int wave = __builtin_amdgcn_readfirstlane(tid >> 6);
  const int wm = wave & 3, wn = wave >> 2;
  const int g = lane >> 4, l15 = lane & 15, q4 = (lane >> 2) & 3, p4 = lane & 3;
  const int row0 = rb * 128;
  const unsigned ldsb = (unsigned)(size_t)lds;

  int baddr[NT];
#pragma unroll
  for (int nt = 0; nt < NT; ++nt) {
    int n0;
    if (TYPE == T_Q) n0 = 28 * (nt >> 1) + 16 * (nt & 1);
    else if (TYPE == T_KV) n0 = (nt < 4 ? 0 : 56) + 28 * ((nt & 3) >> 1) + 16 * (nt & 1);
    else n0 = 16 * nt;
    int byte = 2 * (n0 + 4 * p4);
    int sw = q4 | ((g & 1) << 2);
    baddr[nt] = wn * 16384 + (8 * g + q4) * 256 + (((byte >> 4) ^ (sw << 1)) << 4) + (byte & 15);
  }
  int caddr0 = baddr[0], caddr1 = baddr[1];
#pragma unroll
  for (int w = 1; w < 4; ++w) {
    caddr0 = (wm == w) ? baddr[2 * w] : caddr0;
    caddr1 = (wm == w) ? baddr[(2 * w + 1 < NT) ? 2 * w + 1 : 2 * w] : caddr1;
  }
  const int xrow = wm * 32 + l15;
  const int xa0 = K1_XOFF + xrow * 128 + ((g ^ ((xrow >> 1) & 7)) << 4), xa1 = xa0 ^ 64;
  const int xs0 = xa0 + wn * 2048, xs1 = xa1 + wn * 2048;
  int baddrH[NT], caddr0H = caddr0 + 65536, caddr1H = caddr1 + 65536;
#pragma unroll
  for (int nt = 0; nt < NT; ++nt) { baddrH[nt] = baddr[nt] + 65536; asm volatile("" : "+v"(baddrH[nt])); }
  asm volatile("" : "+v"(caddr0H), "+v"(caddr1H));

  f32x4 acc[2][NT];
#pragma unroll
  for (int mt = 0; mt < 2; ++mt)
#pragma unroll
    for (int nt = 0; nt < NT; ++nt) acc[mt][nt] = f32x4{0.f, 0.f, 0.f, 0.f};
  f32x4 rs_ = {0.f, 0.f, 0.f, 0.f}, rq_ = rs_, cs0 = rs_, cs1 = rs_;
  const u32x4 ONES = {0x3f803f80u, 0x3f803f80u, 0x3f803f80u, 0x3f803f80u};
  STAMP(0, 0);

  struct Frags { u32x4 x0, x1, xs, c0, c1, b[NT]; };
  Frags fr[2];
  auto step = [&](const Frags& f, Frags& n, bool ld, int cn, int ksn, int sc) {
    const int st = cn % 3;
    const int wo = (st == 2 ? 0 : st * K1_WST) + ksn * 8192;
    const int xo = st * K1_XST;
    const int scc = sc < 0 ? 0 : sc;
    const unsigned dx = ldsb + K1_XOFF + (scc % 3) * K1_XST + wave * 1024;
    const unsigned dw = ldsb + (scc % 3) * K1_WST + wave * 1024;
    const char* xb = (const char*)x + scc * 128;
    const char* wb0 = Wh0 + (size_t)scc * 64 * ldw0;
    const char* wb1 = Wh1 + (size_t)scc * 64 * ldw1;
#pragma unroll
    for (int nt = 0; nt < NT; ++nt) {
      if (ld) {
        if (nt == 0) { n.x0 = *(lds_u4*)(lds + (ksn ? xa1 : xa0) + xo); n.x1 = *(lds_u4*)(lds + (ksn ? xa1 : xa0) + xo + 2048); }
        lds_char* bp = lds + (st == 2 ? baddrH[nt] : baddr[nt]) + wo;
        n.b[nt] = tr_read2(bp, bp + 1024);
        if (nt == 3) { lds_char* cp = lds + (st == 2 ? caddr0H : caddr0) + wo; n.c0 = tr_read2(cp, cp + 1024); }
        if (nt == 4) { lds_char* cp = lds + (st == 2 ? caddr1H : caddr1) + wo; n.c1 = tr_read2(cp, cp + 1024); }
        if (nt == 5) n.xs = *(lds_u4*)(lds + (ksn ? xs1 : xs0) + xo);
      }
      const bool trans = (TYPE == T_Q) || (TYPE == T_Z1) || (TYPE == T_KV && nt < 4);
      acc[0][nt] = trans ? mfma16(f.b[nt], f.x0, acc[0][nt]) : mfma16(f.x0, f.b[nt], acc[0][nt]);
      acc[1][nt] = trans ? mfma16(f.b[nt], f.x1, acc[1][nt]) : mfma16(f.x1, f.b[nt], acc[1][nt]);
      if (nt == 1) cs0 = mfma16(ONES, f.c0, cs0);
      if (nt == 2) cs1 = mfma16(ONES, f.c1, cs1);
      if (nt == 3) rs_ = mfma16(ONES, f.xs, rs_);
      if (nt == 4) rq_ = mfma16(f.xs, f.xs, rq_);
      if (sc >= 0 && nt < 6) {
        if (nt < 2) glds16(xb, xoff[nt], dx + nt * 8192);
        else glds16(nt < 4 ? wb0 : wb1, goff[nt - 2], dw + (nt - 2) * 8192);
      }
      __builtin_amdgcn_sched_barrier(0);
    }
  };
  WAIT_VM(12);
  BARRIER();
  STAMP(0, 1);
  {
    Frags& n = fr[0];
    n.x0 = *(lds_u4*)(lds + xa0); n.x1 = *(lds_u4*)(lds + xa0 + 2048);
#pragma unroll
    for (int nt = 0; nt < NT; ++nt) n.b[nt] = tr_read2(lds + baddr[nt], lds + baddr[nt] + 1024);
    n.c0 = tr_read2(lds + caddr0, lds + caddr0 + 1024);
    n.c1 = tr_read2(lds + caddr1, lds + caddr1 + 1024);
    n.xs = *(lds_u4*)(lds + xs0);
  }
#pragma unroll
  for (int c = 0; c < 7; ++c) {
    step(fr[0], fr[1], true, c, 1, -1);
    if (c < 6) {
      if (c < 5) WAIT_VM(6); else WAIT_VM(0);
      WAIT_LGKM0();
      BARRIER();
      STAMP(0, 2 + c);
      if (c == 5 && TYPE == T_KV) {
        unsigned o0 = min(2 * hj * 1568 + wm * 1024 + lane * 16, 25088 - 16);
        glds16(wphi, o0, ldsb + K1_WPHI + wm * 1024);
      }
    }
    step(fr[1], fr[0], c < 6, c + 1, 0, (c + 3 < 7) ? c + 3 : -1);
  }
  STAMP(0, 8);

  lds_f* cstab = (lds_f*)(lds + K1_CS);
  lds_f* rtab = (lds_f*)(lds + K1_RT);
  lds_f* mtab = (lds_f*)(lds + K1_MT);
  {
    float s = rs_[0], c0 = cs0[0], c1 = cs1[0];
    float q = (p4 == 0) ? rq_[0] : (p4 == 1) ? rq_[1] : (p4 == 2) ? rq_[2] : rq_[3];
    q = (g == q4) ? q : 0.f;
    GRP_RED(q, aadd);
    float mean = s * (1.f / 448.f);
    float var = fmaxf(q * (1.f / 448.f) - mean * mean, 0.f);
    float mb = rbf(mean), vb = rbf(var);
    float sd = rbf(__builtin_amdgcn_sqrtf(rbf(vb + 1e-5f)));
    float r = frcp(sd);
    if (g == 0) {
      cstab[(wn * 8 + 2 * wm) * 16 + l15] = c0;
      cstab[(wn * 8 + 2 * wm + 1) * 16 + l15] = c1;
      rtab[wm * 32 + wn * 16 + l15] = r;
      mtab[wm * 32 + wn * 16 + l15] = -mb * r;
    }
  }
  WAIT_VM(0);
  WAIT_LGKM0();
  STAMP(0, 9);
  BARRIER();
  STAMP(0, 10);

  if (TYPE == T_Q || TYPE == T_KV || TYPE == T_Z1) {
    f32x2 Rr[2], Mr[2];
#pragma unroll
    for (int mt = 0; mt < 2; ++mt) {
      float r = rtab[wm * 32 + mt * 16 + l15], m = mtab[wm * 32 + mt * 16 + l15];
      Rr[mt] = f32x2{r, r}; Mr[mt] = f32x2{m, m};
    }
    if (TYPE == T_Z1) {
#pragma unroll
      for (int nt = 0; nt < NT; ++nt) {
        f32x4 c4 = *(lds_f4*)(cstab + (wn * 8 + nt) * 16 + 4 * g);
        const int f = hj * 112 + nt * 16 + 4 * g;
        const int R = (f & 31) >> 3, hh = (f >> 2) & 1;
#pragma unroll
        for (int mt = 0; mt < 2; ++mt) {
          f32x2 a = gelu2(pk_fma(lo2(c4), Mr[mt], lo2(acc[mt][nt]) * Rr[mt]));
          f32x2 b = gelu2(pk_fma(hi2(c4), Mr[mt], hi2(acc[mt][nt]) * Rr[mt]));
          const int s = row0 + wm * 32 + mt * 16 + l15;
          char* dst = ws + WS_Z1B + (size_t)((s >> 5) * 28 + (f >> 5)) * 2048 + R * 512 + ((s & 31) + 32 * hh) * 8;
          *(u32x2*)dst = u32x2{pack2v(a), pack2v(b)};
        }
      }
      BARRIER();
    } else {
      constexpr int NHW = (TYPE == T_Q) ? 4 : 2;
      const bool inval = (g == 3);
      lds_char* scr = lds + wm * 16384;
      f32x4 pp[2][2][2];
#pragma unroll
      for (int hh = 0; hh < NHW; ++hh) {
        const int head = NHW * hj + hh;
        u32x4 a0, a1;
        if (TYPE == T_KV) {
          lds_char* wp = lds + K1_WPHI + hh * 1568 + (4 * g + q4) * 56 + 8 * p4;
          a0 = tr_read2(wp, wp + 16 * 56);
          a1 = tr_read2(wp + 32, wp + 16 * 56 + 32);
        }
        f32x4 c0 = *(lds_f4*)(cstab + (wn * 8 + 2 * hh) * 16 + 4 * g);
        f32x4 c1 = *(lds_f4*)(cstab + (wn * 8 + 2 * hh + 1) * 16 + 4 * g);
#pragma unroll
        for (int mt = 0; mt < 2; ++mt) {
          u32x4 bq;
          bq[0] = pack2v(pk_fma(lo2(c0), Mr[mt], lo2(acc[mt][2 * hh]) * Rr[mt]));
          bq[1] = pack2v(pk_fma(hi2(c0), Mr[mt], hi2(acc[mt][2 * hh]) * Rr[mt]));
          bq[2] = pack2v(pk_fma(lo2(c1), Mr[mt], lo2(acc[mt][2 * hh + 1]) * Rr[mt]));
          bq[3] = pack2v(pk_fma(hi2(c1), Mr[mt], hi2(acc[mt][2 * hh + 1]) * Rr[mt]));
          bq[2] = inval ? 0u : bq[2]; bq[3] = inval ? 0u : bq[3];
          if (TYPE == T_Q) {
            const int stile = rb * 8 + wm * 2 + mt;
            *(u32x4*)(ws + WS_PHIQ + (size_t)(head * 128 + stile) * 1024 + lane * 16) = bq;
          } else {
            f32x4 z4 = {0.f, 0.f, 0.f, 0.f};
            pp[hh & 1][mt][0] = mfma16(a0, bq, z4); pp[hh & 1][mt][1] = mfma16(a1, bq, z4);
          }
        }
      }
      if (TYPE == T_KV) {
        u32x4 vb[2][2];
        f32x4 r4[2], m4[2];
#pragma unroll
        for (int mt = 0; mt < 2; ++mt) {
          r4[mt] = *(lds_f4*)(rtab + wm * 32 + mt * 16 + 4 * g);
          m4[mt] = *(lds_f4*)(mtab + wm * 32 + mt * 16 + 4 * g);
        }
#pragma unroll
        for (int hh = 0; hh < 2; ++hh)
#pragma unroll
          for (int dt = 0; dt < 2; ++dt) {
            const int nt = 4 + 2 * hh + dt;
            float csd = cstab[(wn * 8 + nt) * 16 + l15];
            const f32x2 cc = {csd, csd};
#pragma unroll
            for (int mt = 0; mt < 2; ++mt) {
              vb[hh][dt][2 * mt] = pack2v(pk_fma(cc, lo2(m4[mt]), lo2(acc[mt][nt]) * lo2(r4[mt])));
              vb[hh][dt][2 * mt + 1] = pack2v(pk_fma(cc, hi2(m4[mt]), hi2(acc[mt][nt]) * hi2(r4[mt])));
            }
            if (dt == 1) {
              const unsigned fill = (l15 == 12) ? 0x3f803f80u : 0u;
#pragma unroll
              for (int i = 0; i < 4; ++i) vb[hh][1][i] = (l15 >= 12) ? fill : vb[hh][1][i];
            }
          }
#pragma unroll
        for (int hh = 0; hh < 2; ++hh)
#pragma unroll
          for (int mt = 0; mt < 2; ++mt) {
            u32x4 pos, neg;
            hedgehog_softmax(pp[hh][mt][0], pp[hh][mt][1], inval, pos, neg);
            lds_char* d = scr + hh * 5120 + (mt * 16 + l15) * 160 + 8 * g;
            *(lds_u2*)(d) = u32x2{pos[0], pos[1]};
            *(lds_u2*)(d + 32) = u32x2{pos[2], pos[3]};
            *(lds_u2*)(d + 64) = u32x2{neg[0], neg[1]};
            *(lds_u2*)(d + 96) = u32x2{neg[2], neg[3]};
          }
        f32x4 kv[2][4][2];
#pragma unroll
        for (int hh = 0; hh < 2; ++hh) {
          lds_char* rp = scr + hh * 5120 + (4 * g + q4) * 160 + 8 * p4;
#pragma unroll
          for (int ft = 0; ft < 4; ++ft) {
            u32x4 pa = tr_read2(rp + ft * 32, rp + 16 * 160 + ft * 32);
            f32x4 z4 = {0.f, 0.f, 0.f, 0.f};
            kv[hh][ft][0] = mfma16(pa, vb[hh][0], z4);
            kv[hh][ft][1] = mfma16(pa, vb[hh][1], z4);
          }
        }
        WAIT_LGKM0();
#pragma unroll
        for (int hh = 0; hh < 2; ++hh) {
          lds_char* q = scr + (hh == 0 ? 8192 : 0) + lane * 16;
#pragma unroll
          for (int ft = 0; ft < 4; ++ft) {
            *(lds_f4*)(q + (ft * 2 + 0) * 1024) = kv[hh][ft][0];
            *(lds_f4*)(q + (ft * 2 + 1) * 1024) = kv[hh][ft][1];
          }
        }
      }
      WAIT_LGKM0();
      BARRIER();
      if (TYPE == T_KV) {
#pragma unroll
        for (int it = 0; it < 4; ++it) {
          int pos = it * 256 + tid;
          f32x4 a = *(lds_f4*)(lds + 0 * 16384 + pos * 16);
          a += *(lds_f4*)(lds + 1 * 16384 + pos * 16);
          a += *(lds_f4*)(lds + 2 * 16384 + pos * 16);
          a += *(lds_f4*)(lds + 3 * 16384 + pos * 16);
          int head = 2 * hj + 1 - (pos >> 9);
          *(f32x4*)(ws + WS_KVS + ((size_t)(head * 16 + rb) * 8) * 1024 + (pos & 511) * 16) = a;
        }
      }
    }
  } else {
    lds_f* st = (lds_f*)(lds + K1_ST2);
#pragma unroll
    for (int mt = 0; mt < 2; ++mt) {
      f32x4 r4 = *(lds_f4*)(rtab + wm * 32 + mt * 16 + 4 * g);
      f32x4 m4 = *(lds_f4*)(mtab + wm * 32 + mt * 16 + 4 * g);
      f32x2 sa = {0.f, 0.f}, sb = {0.f, 0.f}, qa = {0.f, 0.f}, qb = {0.f, 0.f};
#pragma unroll
      for (int nt = 0; nt < NT; ++nt) {
        float csd = cstab[(wn * 8 + nt) * 16 + l15];
        const f32x2 cc = {csd, csd};
        f32x2 a = gelu2(pk_fma(cc, lo2(m4), lo2(acc[mt][nt]) * lo2(r4)));
        f32x2 b = gelu2(pk_fma(cc, hi2(m4), hi2(acc[mt][nt]) * hi2(r4)));
        unsigned pa = pack2v(a), pb = pack2v(b);
        sa += a; sb += b; qa = pk_fma(a, a, qa); qb = pk_fma(b, b, qb);
        const int f = hj * 112 + nt * 16 + l15;
        const int s16 = rb * 8 + wm * 2 + mt;
        char* dst = ws + WS_Z2B + (size_t)((f >> 5) * 128 + s16) * 1024 + ((f & 31) + 32 * (g >> 1)) * 16 + (g & 1) * 8;
        *(u32x2*)dst = u32x2{pa, pb};
      }
      f32x4 o0 = {row_sum16(sa[0]), row_sum16(qa[0]), row_sum16(sa[1]), row_sum16(qa[1])};
      f32x4 o1 = {row_sum16(sb[0]), row_sum16(qb[0]), row_sum16(sb[1]), row_sum16(qb[1])};
      if (l15 == 0) {
        *(lds_f4*)(st + (wn * 128 + wm * 32 + mt * 16 + 4 * g) * 2) = o0;
        *(lds_f4*)(st + (wn * 128 + wm * 32 + mt * 16 + 4 * g) * 2 + 4) = o1;
      }
    }
    WAIT_LGKM0();
    BARRIER();
    if (tid < 128) {
      f32x2 a = *(lds_f2*)(st + tid * 2), b = *(lds_f2*)(st + (128 + tid) * 2);
      *(f32x2*)(ws + WS_STAT + ((size_t)(cg - 10) * 2048 + row0 + tid) * 8) = a + b;
    }
  }
}

__global__ __launch_bounds__(512, 2) void k1_kernel(const __bf16* __restrict__ x, const __bf16* __restrict__ wqkv,
                                                    const __bf16* __restrict__ wphi, const __bf16* __restrict__ win,
                                                    const __bf16* __restrict__ wsp, const __bf16* __restrict__ wo,
                                                    const __bf16* __restrict__ wout, char* __restrict__ ws) {
  extern __shared__ __attribute__((aligned(16))) char smem[];
  lds_char* lds = (lds_char*)smem;
  RSTAMP(0, 14);
  const int b = blockIdx.x;
  const int xcd = b & 7, idx = b >> 3;
  const int tid = threadIdx.x, lane = tid & 63;
  if (idx >= 28) {
    const int e = xcd * 4 + (idx - 28);
    unsigned acc = 0;
    const u32x4* p = (const u32x4*)wsp + (size_t)e * 16384 + tid;
#pragma unroll 8
    for (int i = 0; i < 32; ++i) { u32x4 v = p[i * 512]; acc ^= v[0] ^ v[3]; }
    const u32x4* q = (const u32x4*)wout + e * 1568 + tid;
#pragma unroll
    for (int i = 0; i < 4; ++i) if (i * 512 + tid < 1568) { u32x4 v = q[i * 512]; acc ^= v[0] ^ v[3]; }
    const u32x4* r = (const u32x4*)wo + e * 784 + tid;
#pragma unroll
    for (int i = 0; i < 2; ++i) if (i * 512 + tid < 784) { u32x4 v = r[i * 512]; acc ^= v[0] ^ v[3]; }
    asm volatile("" :: "v"(acc));
    return;
  }
  const int rb = 2 * xcd + idx / 14, cg = idx % 14;
  const int wave = __builtin_amdgcn_readfirstlane(tid >> 6);
  const unsigned ldsb = (unsigned)(size_t)lds;
  int ht[2], hj[2];
  k1_half(cg, 0, ht[0], hj[0]);
  k1_half(cg, 1, ht[1], hj[1]);
  unsigned goff[4], xoff[2];
  const char* Wh[2]; int ldw[2];
#pragma unroll
  for (int h = 0; h < 2; ++h) {
    const bool qkv = (ht[h] == T_Q || ht[h] == T_KV);
    Wh[h] = uni_ptr(qkv ? (const char*)wqkv : (const char*)win);
    ldw[h] = __builtin_amdgcn_readfirstlane(qkv ? 2688 : 3584);
#pragma unroll
    for (int i = 0; i < 2; ++i) {
      int row = (i * 8 + wave) * 4 + (lane >> 4);
      int sw = (row & 3) | (((row >> 3) & 1) << 2);
      int c = (lane & 15) ^ (sw << 1);
      int colb;
      if (ht[h] == T_Q) colb = 224 * hj[h] + 16 * min(c, 14);
      else if (ht[h] == T_KV) colb = (c < 7) ? (896 + 112 * hj[h] + 16 * c) : (c < 14 ? (1792 + 112 * hj[h] + 16 * (c - 7)) : (896 + 112 * hj[h]));
      else colb = (ht[h] == T_Z2 ? 1792 : 0) + 224 * hj[h] + 16 * (c < 14 ? c : 0);
      goff[h * 2 + i] = row * ldw[h] + colb;
    }
  }
#pragma unroll
  for (int i = 0; i < 2; ++i) {
    int row = (i * 8 + wave) * 8 + (lane >> 3);
    int c = (lane & 7) ^ ((row >> 1) & 7);
    xoff[i] = (rb * 128 + row) * 896 + c * 16;
  }
#pragma unroll
  for (int chunk = 0; chunk < 3; ++chunk) {
    const unsigned dx = ldsb + K1_XOFF + chunk * K1_XST + wave * 1024;
    const unsigned dw = ldsb + chunk * K1_WST + wave * 1024;
    const char* xb = (const char*)x + chunk * 128;
    glds16(xb, xoff[0], dx);
    glds16(xb, xoff[1], dx + 8192);
#pragma unroll
    for (int i = 0; i < 4; ++i) glds16(Wh[i >> 1] + (size_t)chunk * 64 * ldw[i >> 1], goff[i], dw + i * 8192);
  }
  const int my = (wave >> 2) ? ht[1] : ht[0], mj = (wave >> 2) ? hj[1] : hj[0];
  if (my == T_Q) k1_wave<T_Q>(x, Wh[0], Wh[1], ldw[0], ldw[1], goff, xoff, wphi, ws, lds, rb, cg, mj);
  else if (my == T_KV) k1_wave<T_KV>(x, Wh[0], Wh[1], ldw[0], ldw[1], goff, xoff, wphi, ws, lds, rb, cg, mj);
  else if (my == T_Z1) k1_wave<T_Z1>(x, Wh[0], Wh[1], ldw[0], ldw[1], goff, xoff, wphi, ws, lds, rb, cg, mj);
  else k1_wave<T_Z2>(x, Wh[0], Wh[1], ldw[0], ldw[1], goff, xoff, wphi, ws, lds, rb, cg, mj);
  STAMP(0, 11);
  RSTAMP(0, 15);
}

constexpr int K2_WB = 0;
constexpr int K2_RT = 32768;
constexpr int K2_MT = K2_RT + 8192;
constexpr int K2_CT = K2_MT + 4096;
constexpr int K2_LDS = K2_CT + 256;

__global__ __launch_bounds__(512, 2) void k2_kernel(const __bf16* __restrict__ wsp, const __bf16* __restrict__ bsp,
                                                    const __bf16* __restrict__ wphi, char* __restrict__ ws) {
  extern __shared__ __attribute__((aligned(16))) char smem[];
  lds_char* lds = (lds_char*)smem;
  const int tid = threadIdx.x, lane = tid & 63;
  const int wave = __builtin_amdgcn_readfirstlane(tid >> 6);
  const int b = blockIdx.x, xcd = b & 7, idx = b >> 3;
  RSTAMP(1, 14);
  STAMP(1, 0);
  if (idx >= 28) {
    const int e = xcd * 4 + (idx - 28), head = e >> 1, half = e & 1;
    const int g = lane >> 4, l15 = lane & 15, q4 = (lane >> 2) & 3, p4 = lane & 3;
    u32x4 qf[8];
    const char* pbase = ws + WS_PHIQ + (size_t)(head * 128 + half * 64 + wave * 8) * 1024 + lane * 16;
#pragma unroll
    for (int i = 0; i < 8; ++i) qf[i] = *(const u32x4*)(pbase + i * 1024);
    if (tid < 128) {
      const unsigned o = min(head * 1568 + tid * 16, 25088 - 16);
      *(lds_u4*)(lds + 8192 + tid * 16) = *(const u32x4*)((const char*)wphi + o);
    }
    {
      const char* kb = ws + WS_KVS + ((size_t)(head * 16) * 8 + wave) * 1024 + lane * 16;
      f32x4 a = {0.f, 0.f, 0.f, 0.f};
#pragma unroll
      for (int r = 0; r < 16; ++r) a += *(const f32x4*)(kb + (size_t)r * 8192);
      *(lds_u2*)(lds + wave * 512 + lane * 8) = u32x2{pack2(a[0], a[1]), pack2(a[2], a[3])};
    }
    __syncthreads();
    u32x4 af[2][2];
#pragma unroll
    for (int sg = 0; sg < 2; ++sg)
#pragma unroll
      for (int dt = 0; dt < 2; ++dt) {
        u32x2 lo = *(lds_u2*)(lds + ((2 * sg) * 2 + dt) * 512 + lane * 8);
        u32x2 hi = *(lds_u2*)(lds + ((2 * sg + 1) * 2 + dt) * 512 + lane * 8);
        af[sg][dt] = u32x4{lo[0], lo[1], hi[0], hi[1]};
      }
    lds_char* wp = lds + 8192 + (4 * g + q4) * 56 + 8 * p4;
    const u32x4 a0 = tr_read2(wp, wp + 16 * 56), a1 = tr_read2(wp + 32, wp + 16 * 56 + 32);
    const bool inval = (g == 3);
#pragma unroll
    for (int i = 0; i < 8; ++i) {
      f32x4 z4 = {0.f, 0.f, 0.f, 0.f};
      f32x4 p0 = mfma16(a0, qf[i], z4), p1 = mfma16(a1, qf[i], z4);
      u32x4 pos, neg;
      hedgehog_softmax(p0, p1, inval, pos, neg);
      f32x4 n0 = mfma16(af[0][0], pos, z4); n0 = mfma16(af[1][0], neg, n0);
      f32x4 n1 = mfma16(af[0][1], pos, z4); n1 = mfma16(af[1][1], neg, n1);
      float den = __shfl(n1[0], 48 + l15);
      den = rbf(rbf(den) + 1e-6f);
      float rd = 1.f / den;
      const int s = half * 1024 + (wave * 8 + i) * 16 + l15;
#pragma unroll
      for (int dt = 0; dt < 2; ++dt) {
        const f32x4& n = dt ? n1 : n0;
        u32x2 o = {pack2(rbf(n[0]) * rd, rbf(n[1]) * rd), pack2(rbf(n[2]) * rd, rbf(n[3]) * rd)};
        const int k = 28 * head + 16 * dt + 4 * g;
        const int kl = k & 15, hh = (kl >> 2) & 1, j0 = (kl >> 3) * 4;
        char* dst = ws + WS_HO + (size_t)((s >> 5) * 84 + (k >> 4)) * 1024 + ((s & 31) + 32 * hh) * 16 + j0 * 2;
        if (!(dt == 1 && g == 3)) *(u32x2*)dst = o;
      }
    }
    STAMP(1, 4);
    RSTAMP(1, 15);
    return;
  }

  const int rb = 4 * xcd + idx / 7, ct = idx % 7;
  constexpr int NK = 16;
  const int koff = (ct * 2 + (ct > 3 ? 1 : 0)) & 15;
#define KROT(k) ((((k) + koff) & 15))
  f32x2 sa[4][4];
  auto stat_loads = [&]() {
#pragma unroll
    for (int i = 0; i < 4; ++i)
#pragma unroll
      for (int gq = 0; gq < 4; ++gq) sa[i][gq] = *(const f32x2*)(ws + WS_STAT + ((size_t)gq * 2048 + i * 512 + tid) * 8);
  };
  auto stat_tables = [&]() {
    lds_f* rt = (lds_f*)(lds + K2_RT);
    __attribute__((address_space(3))) unsigned short* mt = (__attribute__((address_space(3))) unsigned short*)(lds + K2_MT);
#pragma unroll
    for (int i = 0; i < 4; ++i) {
      f32x2 t = (sa[i][0] + sa[i][1]) + (sa[i][2] + sa[i][3]);
      float mean = t[0] * (1.f / 896.f);
      float var = fmaxf(t[1] * (1.f / 896.f) - mean * mean, 0.f);
      float vb = rbf(var);
      float sd = rbf(__builtin_amdgcn_sqrtf(rbf(vb + 1e-5f)));
      rt[i * 512 + tid] = frcp(sd);
      mt[i * 512 + tid] = (unsigned short)pack2(mean, 0.f);
    }
  };
  if (wave >= 4) {
    __builtin_amdgcn_s_setprio(2);
    const int pt = tid - 256, t = pt >> 3, cp = pt & 7;
    const int g = lane >> 4, l15 = lane & 15;
    const char* src = (const char*)wsp + (size_t)(rb * 64 + t) * 4096 + cp * 16;
    constexpr int PD = 4;
    u32x4 wr[PD][4];
#define WLOAD(dst, kk) do { _Pragma("unroll") for (int j = 0; j < 4; ++j) \
      dst[j] = *(const u32x4*)(src + KROT(kk) * 256 + (j >> 1) * (32 * 4096) + (j & 1) * 128); } while (0)
    WLOAD(wr[0], 0);
    stat_loads();
    __builtin_amdgcn_sched_barrier(0);
    stat_tables();
    int wa[4];
#pragma unroll
    for (int j = 0; j < 4; ++j) wa[j] = (t + 32 * (j >> 1)) * 256 + (((8 * (j & 1) + cp) ^ (t & 15)) << 4);
    const int ta = cp * 32;
    const int crow = (wave - 4) * 16 + l15;
    const int ca = crow * 256 + ((g ^ l15) << 4);
    f32x4 cacc = {0.f, 0.f, 0.f, 0.f};
    WAIT_LGKM0(); BARRIER();
    f32x4 rr[2][2][2];
#pragma unroll
    for (int j = 0; j < 2; ++j) {
      const int so = (KROT(0) * 128 + j * 64) * 4;
      rr[0][j][0] = *(lds_f4*)(lds + K2_RT + so + ta); rr[0][j][1] = *(lds_f4*)(lds + K2_RT + so + ta + 16);
    }
    WLOAD(wr[1], 1);
    __builtin_amdgcn_sched_barrier(0);
#pragma unroll
    for (int k = 0; k <= NK; ++k) {
      lds_char* wb = lds + K2_WB + (k & 1) * 16384;
      lds_char* pb = lds + K2_WB + ((k + 1) & 1) * 16384;
      u32x4 mf[4], wf[4];
#pragma unroll
      for (int j = 0; j < 4; ++j) {
        if (k > 0) {
          mf[j] = *(lds_u4*)(lds + K2_MT + (KROT(k - 1) * 128 + j * 32 + g * 8) * 2);
          wf[j] = *(lds_u4*)(pb + (ca ^ (j << 6)));
        }
        if (k + 1 < NK && j < 2) {
          const int so = (KROT(k + 1) * 128 + j * 64) * 4;
          rr[(k + 1) & 1][j][0] = *(lds_f4*)(lds + K2_RT + so + ta); rr[(k + 1) & 1][j][1] = *(lds_f4*)(lds + K2_RT + so + ta + 16);
        }
      }
      __builtin_amdgcn_sched_barrier(0);
#pragma unroll
      for (int j = 0; j < 4; ++j) {
        if (k < NK) {
          const f32x4 r0 = rr[k & 1][j & 1][0], r1 = rr[k & 1][j & 1][1];
          u32x4 w = wr[k % PD][j];
          if (k + PD < NK) wr[k % PD][j] = *(const u32x4*)(src + KROT(k + PD) * 256 + (j >> 1) * (32 * 4096) + (j & 1) * 128);
          u32x4 o;
          o[0] = pack2(smul(bf_lo(w[0]), r0[0]), smul(bf_hi(w[0]), r0[1]));
          o[1] = pack2(smul(bf_lo(w[1]), r0[2]), smul(bf_hi(w[1]), r0[3]));
          o[2] = pack2(smul(bf_lo(w[2]), r1[0]), smul(bf_hi(w[2]), r1[1]));
          o[3] = pack2(smul(bf_lo(w[3]), r1[2]), smul(bf_hi(w[3]), r1[3]));
          *(lds_u4*)(wb + wa[j]) = o;
        }
        __builtin_amdgcn_sched_barrier(0);
        if (k > 0) cacc = mfma16(mf[j], wf[j], cacc);
      }
      if (k == 0) { WLOAD(wr[2], 2); WLOAD(wr[3], 3); }
      if (k == NK) {
        if (g == 0) *(lds_f*)(lds + K2_CT + crow * 4) = cacc[0];
      }
      WAIT_LGKM0();
      BARRIER();
    }
  } else {
    const int F = 4 * ct + wave, l31 = lane & 31, hh = lane >> 5;
    const char* zsrc = ws + WS_Z2B + (size_t)F * 128 * 1024 + lane * 16;
    constexpr int ZD = 24;
    u32x4 zf[ZD];
#pragma unroll
    for (int i = 0; i < 8; ++i) zf[i] = *(const u32x4*)(zsrc + (KROT(i >> 3) * 8 + (i & 7)) * 1024);
    stat_loads();
    __builtin_amdgcn_sched_barrier(0);
    stat_tables();
    WAIT_LGKM0(); BARRIER();
    STAMP(1, 1);
    const int ra = l31 * 256 + ((hh ^ (l31 & 15)) << 4);
    f32x16 acc[2];
#pragma unroll
    for (int i = 0; i < 16; ++i) { acc[0][i] = 0.f; acc[1][i] = 0.f; }
    u32x2 z1v[2][4];
    float bb[2];
    BARRIER();
    STAMP(1, 2);
    int rk[8];
#pragma unroll
    for (int kk = 0; kk < 8; ++kk) { rk[kk] = ra ^ (kk << 5); }
    u32x4 w0 = *(lds_u4*)(lds + rk[0]), w1 = *(lds_u4*)(lds + rk[0] + 8192);
#pragma unroll
    for (int k = 0; k < NK; ++k) {
#pragma unroll
      for (int kk = 0; kk < 8; ++kk) {
        const int ks = k * 8 + kk;
        u32x4 n0 = w0, n1 = w1;
        if (kk == 7 && k + 1 < NK) { WAIT_LGKM0(); BARRIER(); }
        if (ks + 1 < 128) {
          const int nb = (((ks + 1) >> 3) & 1) * 16384;
          n0 = *(lds_u4*)(lds + rk[(kk + 1) & 7] + nb); n1 = *(lds_u4*)(lds + rk[(kk + 1) & 7] + nb + 8192);
        }
        if (ks == 108) {
#pragma unroll
          for (int tt = 0; tt < 2; ++tt) {
            const int t = rb * 64 + tt * 32 + l31;
            bb[tt] = bf_lo((unsigned)*(const unsigned short*)(bsp + t));
            const char* z1p = ws + WS_Z1B + (size_t)((t >> 5) * 28 + F) * 2048 + lane * 8;
#pragma unroll
            for (int R = 0; R < 4; ++R) z1v[tt][R] = *(const u32x2*)(z1p + R * 512);
          }
        }
        u32x4 z = zf[ks % ZD];
#define ZLOAD(i) zf[(i) % ZD] = *(const u32x4*)(zsrc + (KROT((i) >> 3) * 8 + ((i) & 7)) * 1024)
        if (ks < 16) { ZLOAD(8 + 2 * ks); ZLOAD(9 + 2 * ks); }
        else if (ks + ZD < 128) ZLOAD(ks + ZD);
        acc[0] = mfma32(z, w0, acc[0]);
        acc[1] = mfma32(z, w1, acc[1]);
        w0 = n0; w1 = n1;
        __builtin_amdgcn_sched_barrier(0);
      }
    }
    WAIT_LGKM0();
    BARRIER();
    STAMP(1, 3);
#pragma unroll
    for (int tt = 0; tt < 2; ++tt) {
      const int t = rb * 64 + tt * 32 + l31;
      const float c = *(lds_f*)(lds + K2_CT + (tt * 32 + l31) * 4);
      const f32x2 cc = {c, c}, b2 = {bb[tt], bb[tt]};
      unsigned hp[8];
#pragma unroll
      for (int R = 0; R < 4; ++R) {
        f32x2 g0 = unpack2(pack2v(f32x2{acc[tt][4 * R + 0], acc[tt][4 * R + 1]} - cc));
        f32x2 g1 = unpack2(pack2v(f32x2{acc[tt][4 * R + 2], acc[tt][4 * R + 3]} - cc));
        g0 = unpack2(pack2v(g0 + b2)); g1 = unpack2(pack2v(g1 + b2));
        hp[2 * R] = pack2v(unpack2(z1v[tt][R][0]) * g0); hp[2 * R + 1] = pack2v(unpack2(z1v[tt][R][1]) * g1);
      }
      char* dst = ws + WS_HO + (size_t)((t >> 5) * 84 + 28 + 2 * F) * 1024 + lane * 16;
      *(u32x4*)dst = u32x4{hp[0], hp[1], hp[2], hp[3]};
      *(u32x4*)(dst + 1024) = u32x4{hp[4], hp[5], hp[6], hp[7]};
    }
    STAMP(1, 4);
    RSTAMP(1, 15);
  }
}

constexpr int K3_STG = 16384;
constexpr int K3_NB = 5;
constexpr int K3_LDS = K3_STG * K3_NB;

__global__ __launch_bounds__(256, 2) void k3_kernel(const __bf16* __restrict__ x, const __bf16* __restrict__ wo,
                                                    const __bf16* __restrict__ wout, char* __restrict__ ws,
                                                    __bf16* __restrict__ out) {
  extern __shared__ __attribute__((aligned(16))) char smem[];
  lds_char* lds = (lds_char*)smem;
  const unsigned ldsb = (unsigned)(size_t)lds;
  const int tid = threadIdx.x, lane = tid & 63;
  const int wave = __builtin_amdgcn_readfirstlane(tid >> 6);
  const int b = blockIdx.x, xcd = b & 7, idx = b >> 3;
  const int rb = 4 * xcd + idx / 7, ct = idx % 7;
  const int wt = wave & 1, wn = wave >> 1;
  const int l31 = lane & 31, hh = lane >> 5, gi = lane >> 4, q4 = (lane >> 2) & 3, p4 = lane & 3;
  RSTAMP(2, 14);
  STAMP(2, 0);

  unsigned goff[2];
#pragma unroll
  for (int i = 0; i < 2; ++i) {
    int row = (i * 4 + wave) * 8 + (lane >> 3);
    int sw = ((row & 3) << 1) ^ (((row >> 3) & 1));
    int c = (lane & 7) ^ sw;
    goff[i] = row * 896 + ct * 128 + c * 16;
  }
  const char* abase = ws + WS_HO + (size_t)(rb * 2) * 84 * 1024;
  const unsigned aoff = wave * 1024 + lane * 16;
  auto stage = [&](int chunk) {
    const char* base = (chunk < 7) ? ((const char*)wo + (size_t)chunk * 64 * 896) : ((const char*)wout + (size_t)(chunk - 7) * 64 * 896);
    const unsigned dst = ldsb + (chunk % K3_NB) * K3_STG + wave * 1024;
    glds16(abase + chunk * 4096, aoff, dst);
    glds16(abase + chunk * 4096 + 84 * 1024, aoff, dst + 4096);
    glds16(base, goff[0], dst + 8192);
    glds16(base, goff[1], dst + 8192 + 4096);
  };
  const int t = rb * 64 + wt * 32 + l31;
  u32x2 xv[4];
  {
    const unsigned xo = (unsigned)(t * D_ + ct * 64 + wn * 32 + 4 * hh) * 2;
    asm volatile("global_load_dwordx2 %0, %4, %5\n\tglobal_load_dwordx2 %1, %4, %5 offset:16\n\t"
                 "global_load_dwordx2 %2, %4, %5 offset:32\n\tglobal_load_dwordx2 %3, %4, %5 offset:48"
                 : "=&v"(xv[0]), "=&v"(xv[1]), "=&v"(xv[2]), "=&v"(xv[3]) : "v"(xo), "s"(x) : "memory");
  }
  stage(0); stage(1); stage(2); stage(3);

  int baddr;
  {
    int row = 4 * (gi >> 1) + q4;
    int byte = 2 * (wn * 32 + (gi & 1) * 16 + 4 * p4);
    int sw = ((row & 3) << 1);
    baddr = 8192 + row * 128 + (((byte >> 4) ^ sw) << 4) + (byte & 15);
  }
  const int baddr2 = (baddr ^ 16) + 8 * 128;
  const int aaddr = wt * 4096 + lane * 16;

  f32x16 acc;
#pragma unroll
  for (int i = 0; i < 16; ++i) acc[i] = 0.f;
  unsigned t1[8];
  asm volatile("s_waitcnt vmcnt(12)" : "+v"(xv[0]), "+v"(xv[1]), "+v"(xv[2]), "+v"(xv[3]) :: "memory");
  BARRIER();
  STAMP(2, 1);
  u32x4 wf = tr_read2(lds + baddr, lds + baddr2), af = *(lds_u4*)(lds + aaddr);
#pragma unroll
  for (int c = 0; c < 21; ++c) {
#pragma unroll
    for (int kk = 0; kk < 4; ++kk) {
      u32x4 wn_ = wf, an_ = af;
      if (kk == 3 && c + 1 < 21) {
        if (c + 3 < 21) WAIT_VM(8); else if (c + 2 < 21) WAIT_VM(4); else WAIT_VM(0);
        WAIT_LGKM0();
        BARRIER();
        if (c + 4 < 21) stage(c + 4);
      }
      if (c * 4 + kk + 1 < 84) {
        const int cn = (kk == 3) ? c + 1 : c, kn = (kk + 1) & 3;
        lds_char* bufp = lds + (cn % K3_NB) * K3_STG;
        wn_ = tr_read2(bufp + baddr + kn * 2048, bufp + baddr2 + kn * 2048);
        an_ = *(lds_u4*)(bufp + aaddr + kn * 1024);
      }
      acc = mfma32(wf, af, acc);
      wf = wn_; af = an_;
      __builtin_amdgcn_sched_barrier(0);
    }
    if (c == 6) {
#pragma unroll
      for (int R = 0; R < 4; ++R) {
        f32x2 a0 = unpack2(pack2v(f32x2{acc[4 * R + 0], acc[4 * R + 1]}));
        f32x2 a1 = unpack2(pack2v(f32x2{acc[4 * R + 2], acc[4 * R + 3]}));
        t1[2 * R] = pack2v(unpack2(xv[R][0]) + a0); t1[2 * R + 1] = pack2v(unpack2(xv[R][1]) + a1);
      }
#pragma unroll
      for (int i = 0; i < 16; ++i) acc[i] = 0.f;
    }
  }
  STAMP(2, 2);
  __bf16* orow = out + (size_t)t * D_ + ct * 64 + wn * 32 + 4 * hh;
#pragma unroll
  for (int R = 0; R < 4; ++R) {
    f32x2 a0 = unpack2(pack2v(f32x2{acc[4 * R + 0], acc[4 * R + 1]}));
    f32x2 a1 = unpack2(pack2v(f32x2{acc[4 * R + 2], acc[4 * R + 3]}));
    *(u32x2*)(orow + 8 * R) = u32x2{pack2v(unpack2(t1[2 * R]) + a0), pack2v(unpack2(t1[2 * R + 1]) + a1)};
  }
  STAMP(2, 3);
  RSTAMP(2, 15);
}

extern "C" void kernel_launch(void* const* d_in, const int* in_sizes, int n_in, void* d_out, int out_size,
                              void* d_ws, size_t ws_size, hipStream_t stream) {
  const __bf16* x = (const __bf16*)d_in[0];
  const __bf16* wqkv = (const __bf16*)d_in[1];
  const __bf16* wphi = (const __bf16*)d_in[2];
  const __bf16* wo = (const __bf16*)d_in[3];
  const __bf16* win = (const __bf16*)d_in[5];
  const __bf16* wsp = (const __bf16*)d_in[6];
  const __bf16* bsp = (const __bf16*)d_in[7];
  const __bf16* wout = (const __bf16*)d_in[8];
  char* ws = (char*)d_ws;
  (void)hipFuncSetAttribute((const void*)k1_kernel, hipFuncAttributeMaxDynamicSharedMemorySize, K1_LDS);
  (void)hipFuncSetAttribute((const void*)k3_kernel, hipFuncAttributeMaxDynamicSharedMemorySize, K3_LDS);
  k1_kernel<<<256, 512, K1_LDS, stream>>>(x, wqkv, wphi, win, wsp, wo, wout, ws);
  k2_kernel<<<256, 512, K2_LDS, stream>>>(wsp, bsp, wphi, ws);
  k3_kernel<<<224, 256, K3_LDS, stream>>>(x, wo, wout, ws, (__bf16*)d_out);
}
```

```cpp
#include <hip/hip_runtime.h>
#include <hip/hip_bf16.h>
#include <stdint.h>

#define DI __device__ __forceinline__
typedef __attribute__((ext_vector_type(8))) __bf16 bf16x8;
typedef __attribute__((ext_vector_type(2))) __bf16 bf16x2;
typedef __attribute__((ext_vector_type(4))) short s16x4;
typedef __attribute__((ext_vector_type(4))) float f32x4;
typedef __attribute__((ext_vector_type(2))) float f32x2;
typedef __attribute__((ext_vector_type(16))) float f32x16;
typedef __attribute__((ext_vector_type(4))) unsigned u32x4;
typedef __attribute__((ext_vector_type(2))) unsigned u32x2;
typedef __attribute__((address_space(3))) char lds_char;
typedef __attribute__((address_space(3))) s16x4 lds_s16x4;

constexpr int S_ = 2048, D_ = 448;

constexpr size_t WS_PHIQ = 0;
constexpr size_t WS_KVS = 4u << 20;
constexpr size_t WS_Z1B = 6u << 20;
constexpr size_t WS_Z2B = 10u << 20;
constexpr size_t WS_STAT = 14u << 20;
constexpr size_t WS_HO = 15u << 20;

DI unsigned pack2(float a, float b) {
  f32x2 v = {a, b};
  return __builtin_bit_cast(unsigned, __builtin_convertvector(v, bf16x2));
}
DI float bf_lo(unsigned u) { return __uint_as_float(u << 16); }
DI float bf_hi(unsigned u) { return __uint_as_float(u & 0xffff0000u); }
DI float rbf(float x) { return bf_lo(pack2(x, 0.f)); }
DI float dot2(unsigned a, unsigned b, float c) {
  return __builtin_amdgcn_fdot2_f32_bf16(__builtin_bit_cast(bf16x2, a), __builtin_bit_cast(bf16x2, b), c, false);
}
DI f32x4 mfma16(u32x4 a, u32x4 b, f32x4 c) {
  return __builtin_amdgcn_mfma_f32_16x16x32_bf16(__builtin_bit_cast(bf16x8, a), __builtin_bit_cast(bf16x8, b), c, 0, 0, 0);
}
DI f32x16 mfma32(u32x4 a, u32x4 b, f32x16 c) {
  return __builtin_amdgcn_mfma_f32_32x32x16_bf16(__builtin_bit_cast(bf16x8, a), __builtin_bit_cast(bf16x8, b), c, 0, 0, 0);
}
DI u32x2 tr_read(lds_char* p) {
  s16x4 r = __builtin_amdgcn_ds_read_tr16_b64_v4i16((lds_s16x4*)p);
  return __builtin_bit_cast(u32x2, r);
}
DI u32x4 tr_read2(lds_char* p0, lds_char* p1) {
  u32x2 a = tr_read(p0), b = tr_read(p1);
  u32x4 r = {a[0], a[1], b[0], b[1]};
  return r;
}
DI float fexp(float x) { return __builtin_amdgcn_exp2f(x * 1.4426950408889634f); }
DI float frcp(float x) { return __builtin_amdgcn_rcpf(x); }
DI float gelu_tanh(float x) {
  float u = 0.7978845608028654f * (x + 0.044715f * x * x * x);
  float e = __builtin_amdgcn_exp2f(-2.885390081777927f * u);
  return x * frcp(1.f + e);
}
DI const char* uni_ptr(const char* p) {
  unsigned long long v = (unsigned long long)p;
  unsigned lo = __builtin_amdgcn_readfirstlane((unsigned)v), hi = __builtin_amdgcn_readfirstlane((unsigned)(v >> 32));
  return (const char*)(((unsigned long long)hi << 32) | lo);
}
DI void glds16(const void* sbase, unsigned voff, unsigned lds_dst) {
  asm volatile("s_mov_b32 m0, %2\n\ts_nop 0\n\tglobal_load_lds_dwordx4 %0, %1"
               :: "v"(voff), "s"(sbase), "s"(lds_dst) : "memory");
}
#define STAMP(kid, i)
#define RSTAMP(kid, i)
#define STMODE 3
DI void st16(void* p, u32x4 v) {
  asm volatile("global_store_dwordx4 %0, %1, off sc0 sc1\n\ts_nop 1" :: "v"(p), "v"(v) : "memory");
}
DI void st8(void* p, u32x2 v) {
  asm volatile("global_store_dwordx2 %0, %1, off sc0 sc1\n\ts_nop 1" :: "v"(p), "v"(v) : "memory");
}
#define WAIT_VMI(n) asm volatile("s_waitcnt vmcnt(%0)" :: "i"(n) : "memory")
#define WAIT_VM(n) asm volatile("s_waitcnt vmcnt(" #n ")" ::: "memory")
#define WAIT_LGKM0() asm volatile("s_waitcnt lgkmcnt(0)" ::: "memory")
#define BARRIER() __builtin_amdgcn_s_barrier()

constexpr int K1_WST = 32768;
constexpr int K1_XOFF = 98304;
constexpr int K1_XST = 16384;
constexpr int K1_WPHI = 131072;
constexpr int K1_CS = K1_WPHI + 13312;
constexpr int K1_RT = K1_CS + 1024;
constexpr int K1_MT = K1_RT + 512;
constexpr int K1_ST2 = K1_MT + 512;
constexpr int K1_LDS = 147456 + 2048;

enum { T_Q = 0, T_KV = 1, T_Z1 = 2, T_Z2 = 3 };

typedef __attribute__((address_space(3))) float lds_f;
typedef __attribute__((address_space(3))) f32x4 lds_f4;
typedef __attribute__((address_space(3))) f32x2 lds_f2;
typedef __attribute__((address_space(3))) u32x4 lds_u4;
typedef __attribute__((address_space(3))) u32x2 lds_u2;

DI float amax(float a, float b) { float r; asm("v_max_f32 %0, %1, %2" : "=v"(r) : "v"(a), "v"(b)); return r; }
DI float amin(float a, float b) { float r; asm("v_min_f32 %0, %1, %2" : "=v"(r) : "v"(a), "v"(b)); return r; }
#define GRP_RED(x, OP) do { \
    auto r_ = __builtin_amdgcn_permlane16_swap(__float_as_uint(x), __float_as_uint(x), false, false); \
    x = OP(__uint_as_float(r_[0]), __uint_as_float(r_[1])); \
    auto q_ = __builtin_amdgcn_permlane32_swap(__float_as_uint(x), __float_as_uint(x), false, false); \
    x = OP(__uint_as_float(q_[0]), __uint_as_float(q_[1])); } while (0)
DI float aadd(float a, float b) { return a + b; }
DI float smul(float a, float b) { float r; asm("v_mul_f32 %0, %1, %2" : "=v"(r) : "v"(a), "v"(b)); return r; }
DI f32x2 pk_fma(f32x2 a, f32x2 b, f32x2 c) { return __builtin_elementwise_fma(a, b, c); }
DI f32x2 lo2(const f32x4& v) { return f32x2{v[0], v[1]}; }
DI f32x2 hi2(const f32x4& v) { return f32x2{v[2], v[3]}; }
DI unsigned pack2v(f32x2 v) { return __builtin_bit_cast(unsigned, __builtin_convertvector(v, bf16x2)); }
DI f32x2 unpack2(unsigned u) { return f32x2{bf_lo(u), bf_hi(u)}; }
DI f32x2 gelu2(f32x2 v) {
  const f32x2 c1 = {-2.302208198f, -2.302208198f};
  const f32x2 c2 = {-0.1029432397f, -0.1029432397f};
  f32x2 u = v * pk_fma(v * v, c2, c1);
  f32x2 d = f32x2{__builtin_amdgcn_exp2f(u[0]), __builtin_amdgcn_exp2f(u[1])} + f32x2{1.f, 1.f};
  return v * f32x2{frcp(d[0]), frcp(d[1])};
}
DI float row_sum16(float x) {
  x += __builtin_bit_cast(float, __builtin_amdgcn_update_dpp(0, __builtin_bit_cast(int, x), 0x128, 0xf, 0xf, false));
  x += __builtin_bit_cast(float, __builtin_amdgcn_update_dpp(0, __builtin_bit_cast(int, x), 0x124, 0xf, 0xf, false));
  x += __builtin_bit_cast(float, __builtin_amdgcn_update_dpp(0, __builtin_bit_cast(int, x), 0x122, 0xf, 0xf, false));
  x += __builtin_bit_cast(float, __builtin_amdgcn_update_dpp(0, __builtin_bit_cast(int, x), 0x121, 0xf, 0xf, false));
  return x;
}

DI void hedgehog_softmax(const f32x4& p0, const f32x4& p1, bool inval, u32x4& pos, u32x4& neg) {
  f32x2 v[4];
  v[0] = unpack2(pack2v(lo2(p0))); v[1] = unpack2(pack2v(hi2(p0)));
  v[2] = unpack2(pack2v(lo2(p1))); v[3] = unpack2(pack2v(hi2(p1)));
  f32x2 w2 = inval ? v[0] : v[2], w3 = inval ? v[1] : v[3];
  float mx = __builtin_fmaxf(__builtin_fmaxf(__builtin_fmaxf(v[0][0], v[0][1]), __builtin_fmaxf(v[1][0], v[1][1])),
                             __builtin_fmaxf(__builtin_fmaxf(w2[0], w2[1]), __builtin_fmaxf(w3[0], w3[1])));
  float mn = __builtin_fminf(__builtin_fminf(__builtin_fminf(v[0][0], v[0][1]), __builtin_fminf(v[1][0], v[1][1])),
                             __builtin_fminf(__builtin_fminf(w2[0], w2[1]), __builtin_fminf(w3[0], w3[1])));
  GRP_RED(mx, amax);
  GRP_RED(mn, amin);
  const float L = 1.4426950408889634f;
  const f32x2 L2 = {L, L}, nL2 = {-L, -L};
  const f32x2 bx = {-mx * L, -mx * L}, bn = {mn * L, mn * L};
  f32x2 e1[4], e2[4];
#pragma unroll
  for (int i = 0; i < 4; ++i) {
    f32x2 a = pk_fma(v[i], L2, bx), b = pk_fma(v[i], nL2, bn);
    e1[i] = f32x2{__builtin_amdgcn_exp2f(a[0]), __builtin_amdgcn_exp2f(a[1])};
    e2[i] = f32x2{__builtin_amdgcn_exp2f(b[0]), __builtin_amdgcn_exp2f(b[1])};
  }
  const f32x2 zz = {0.f, 0.f};
  e1[2] = inval ? zz : e1[2]; e1[3] = inval ? zz : e1[3];
  e2[2] = inval ? zz : e2[2]; e2[3] = inval ? zz : e2[3];
  f32x2 t1 = (e1[0] + e1[1]) + (e1[2] + e1[3]), t2 = (e2[0] + e2[1]) + (e2[2] + e2[3]);
  float s1 = t1[0] + t1[1], s2 = t2[0] + t2[1];
  GRP_RED(s1, aadd);
  GRP_RED(s2, aadd);
  float r1 = frcp(s1), r2 = frcp(s2);
  const f32x2 R1 = {r1, r1}, R2 = {r2, r2};
#pragma unroll
  for (int i = 0; i < 4; ++i) { pos[i] = pack2v(e1[i] * R1); neg[i] = pack2v(e2[i] * R2); }
}

DI void k1_half(int cg, int wn, int& ht, int& j) {
  if (cg < 8) { if (wn == 0) { ht = T_KV; j = cg; } else if (cg < 4) { ht = T_Q; j = cg; } else { ht = T_Z1; j = cg - 4; } }
  else if (cg < 10) { ht = T_Z1; j = 4 + 2 * (cg - 8) + wn; }
  else { ht = T_Z2; j = 2 * (cg - 10) + wn; }
}

template <int TYPE>
DI void k1_wave(const __bf16* __restrict__ x, const char* Wh0, const char* Wh1, int ldw0, int ldw1,
                const unsigned (&goff)[4], const unsigned (&xoff)[2], const __bf16* __restrict__ wphi,
                char* __restrict__ ws, lds_char* lds, int rb, int cg, int hj) {
  constexpr int NT = (TYPE == T_Q || TYPE == T_KV) ? 8 : 7;
  const int tid = threadIdx.x, lane = tid & 63;
  const int wave = __builtin_amdgcn_readfirstlane(tid >> 6);
  const int wm = wave & 3, wn = wave >> 2;
  const int g = lane >> 4, l15 = lane & 15, q4 = (lane >> 2) & 3, p4 = lane & 3;
  const int row0 = rb * 128;
  const unsigned ldsb = (unsigned)(size_t)lds;

  int baddr[NT];
#pragma unroll
  for (int nt = 0; nt < NT; ++nt) {
    int n0;
    if (TYPE == T_Q) n0 = 28 * (nt >> 1) + 16 * (nt & 1);
    else if (TYPE == T_KV) n0 = (nt < 4 ? 0 : 56) + 28 * ((nt & 3) >> 1) + 16 * (nt & 1);
    else n0 = 16 * nt;
    int byte = 2 * (n0 + 4 * p4);
    int sw = q4 | ((g & 1) << 2);
    baddr[nt] = wn * 16384 + (8 * g + q4) * 256 + (((byte >> 4) ^ (sw << 1)) << 4) + (byte & 15);
  }
  int caddr0 = baddr[0], caddr1 = baddr[1];
#pragma unroll
  for (int w = 1; w < 4; ++w) {
    caddr0 = (wm == w) ? baddr[2 * w] : caddr0;
    caddr1 = (wm == w) ? baddr[(2 * w + 1 < NT) ? 2 * w + 1 : 2 * w] : caddr1;
  }
  const int xrow = wm * 32 + l15;
  const int xa0 = K1_XOFF + xrow * 128 + ((g ^ ((xrow >> 1) & 7)) << 4), xa1 = xa0 ^ 64;
  const int xs0 = xa0 + wn * 2048, xs1 = xa1 + wn * 2048;
  int baddrH[NT], caddr0H = caddr0 + 65536, caddr1H = caddr1 + 65536;
#pragma unroll
  for (int nt = 0; nt < NT; ++nt) { baddrH[nt] = baddr[nt] + 65536; asm volatile("" : "+v"(baddrH[nt])); }
  asm volatile("" : "+v"(caddr0H), "+v"(caddr1H));

  f32x4 acc[2][NT];
#pragma unroll
  for (int mt = 0; mt < 2; ++mt)
#pragma unroll
    for (int nt = 0; nt < NT; ++nt) acc[mt][nt] = f32x4{0.f, 0.f, 0.f, 0.f};
  f32x4 rs_ = {0.f, 0.f, 0.f, 0.f}, rq_ = rs_, cs0 = rs_, cs1 = rs_;
  const u32x4 ONES = {0x3f803f80u, 0x3f803f80u, 0x3f803f80u, 0x3f803f80u};
  STAMP(0, 0);

  struct Frags { u32x4 x0, x1, xs, c0, c1, b[NT]; };
  Frags fr[2];
  auto step = [&](const Frags& f, Frags& n, bool ld, int cn, int ksn, int sc) {
    const int st = cn % 3;
    const int wo = (st == 2 ? 0 : st * K1_WST) + ksn * 8192;
    const int xo = st * K1_XST;
    const int scc = sc < 0 ? 0 : sc;
    const unsigned dx = ldsb + K1_XOFF + (scc % 3) * K1_XST + wave * 1024;
    const unsigned dw = ldsb + (scc % 3) * K1_WST + wave * 1024;
    const char* xb = (const char*)x + scc * 128;
    const char* wb0 = Wh0 + (size_t)scc * 64 * ldw0;
    const char* wb1 = Wh1 + (size_t)scc * 64 * ldw1;
#pragma unroll
    for (int nt = 0; nt < NT; ++nt) {
      if (ld) {
        if (nt == 0) { n.x0 = *(lds_u4*)(lds + (ksn ? xa1 : xa0) + xo); n.x1 = *(lds_u4*)(lds + (ksn ? xa1 : xa0) + xo + 2048); }
        lds_char* bp = lds + (st == 2 ? baddrH[nt] : baddr[nt]) + wo;
        n.b[nt] = tr_read2(bp, bp + 1024);
        if (nt == 3) { lds_char* cp = lds + (st == 2 ? caddr0H : caddr0) + wo; n.c0 = tr_read2(cp, cp + 1024); }
        if (nt == 4) { lds_char* cp = lds + (st == 2 ? caddr1H : caddr1) + wo; n.c1 = tr_read2(cp, cp + 1024); }
        if (nt == 5) n.xs = *(lds_u4*)(lds + (ksn ? xs1 : xs0) + xo);
      }
      const bool trans = (TYPE == T_Q) || (TYPE == T_Z1) || (TYPE == T_KV && nt < 4);
      acc[0][nt] = trans ? mfma16(f.b[nt], f.x0, acc[0][nt]) : mfma16(f.x0, f.b[nt], acc[0][nt]);
      acc[1][nt] = trans ? mfma16(f.b[nt], f.x1, acc[1][nt]) : mfma16(f.x1, f.b[nt], acc[1][nt]);
      if (nt == 1) cs0 = mfma16(ONES, f.c0, cs0);
      if (nt == 2) cs1 = mfma16(ONES, f.c1, cs1);
      if (nt == 3) rs_ = mfma16(ONES, f.xs, rs_);
      if (nt == 4) rq_ = mfma16(f.xs, f.xs, rq_);
      if (sc >= 0 && nt < 6) {
        if (nt < 2) glds16(xb, xoff[nt], dx + nt * 8192);
        else glds16(nt < 4 ? wb0 : wb1, goff[nt - 2], dw + (nt - 2) * 8192);
      }
      __builtin_amdgcn_sched_barrier(0);
    }
  };
  WAIT_VM(12);
  BARRIER();
  STAMP(0, 1);
  {
    Frags& n = fr[0];
    n.x0 = *(lds_u4*)(lds + xa0); n.x1 = *(lds_u4*)(lds + xa0 + 2048);
#pragma unroll
    for (int nt = 0; nt < NT; ++nt) n.b[nt] = tr_read2(lds + baddr[nt], lds + baddr[nt] + 1024);
    n.c0 = tr_read2(lds + caddr0, lds + caddr0 + 1024);
    n.c1 = tr_read2(lds + caddr1, lds + caddr1 + 1024);
    n.xs = *(lds_u4*)(lds + xs0);
  }
#pragma unroll
  for (int c = 0; c < 7; ++c) {
    step(fr[0], fr[1], true, c, 1, -1);
    if (c < 6) {
      if (c < 5) WAIT_VM(6); else WAIT_VM(0);
      WAIT_LGKM0();
      BARRIER();
      STAMP(0, 2 + c);
      if (c == 5 && TYPE == T_KV) {
        unsigned o0 = min(2 * hj * 1568 + wm * 1024 + lane * 16, 25088 - 16);
        glds16(wphi, o0, ldsb + K1_WPHI + wm * 1024);
      }
    }
    step(fr[1], fr[0], c < 6, c + 1, 0, (c + 3 < 7) ? c + 3 : -1);
  }
  STAMP(0, 8);

  lds_f* cstab = (lds_f*)(lds + K1_CS);
  lds_f* rtab = (lds_f*)(lds + K1_RT);
  lds_f* mtab = (lds_f*)(lds + K1_MT);
  {
    float s = rs_[0], c0 = cs0[0], c1 = cs1[0];
    float q = (p4 == 0) ? rq_[0] : (p4 == 1) ? rq_[1] : (p4 == 2) ? rq_[2] : rq_[3];
    q = (g == q4) ? q : 0.f;
    GRP_RED(q, aadd);
    float mean = s * (1.f / 448.f);
    float var = fmaxf(q * (1.f / 448.f) - mean * mean, 0.f);
    float mb = rbf(mean), vb = rbf(var);
    float sd = rbf(__builtin_amdgcn_sqrtf(rbf(vb + 1e-5f)));
    float r = frcp(sd);
    if (g == 0) {
      cstab[(wn * 8 + 2 * wm) * 16 + l15] = c0;
      cstab[(wn * 8 + 2 * wm + 1) * 16 + l15] = c1;
      rtab[wm * 32 + wn * 16 + l15] = r;
      mtab[wm * 32 + wn * 16 + l15] = -mb * r;
    }
  }
  WAIT_VM(0);
  WAIT_LGKM0();
  STAMP(0, 9);
  BARRIER();
  STAMP(0, 10);

  if (TYPE == T_Q || TYPE == T_KV || TYPE == T_Z1) {
    f32x2 Rr[2], Mr[2];
#pragma unroll
    for (int mt = 0; mt < 2; ++mt) {
      float r = rtab[wm * 32 + mt * 16 + l15], m = mtab[wm * 32 + mt * 16 + l15];
      Rr[mt] = f32x2{r, r}; Mr[mt] = f32x2{m, m};
    }
    if (TYPE == T_Z1) {
#pragma unroll
      for (int nt = 0; nt < NT; ++nt) {
        f32x4 c4 = *(lds_f4*)(cstab + (wn * 8 + nt) * 16 + 4 * g);
        const int f = hj * 112 + nt * 16 + 4 * g;
        const int R = (f & 31) >> 3, hh = (f >> 2) & 1;
#pragma unroll
        for (int mt = 0; mt < 2; ++mt) {
          f32x2 a = gelu2(pk_fma(lo2(c4), Mr[mt], lo2(acc[mt][nt]) * Rr[mt]));
          f32x2 b = gelu2(pk_fma(hi2(c4), Mr[mt], hi2(acc[mt][nt]) * Rr[mt]));
          const int s = row0 + wm * 32 + mt * 16 + l15;
          char* dst = ws + WS_Z1B + (size_t)((s >> 5) * 28 + (f >> 5)) * 2048 + R * 512 + ((s & 31) + 32 * hh) * 8;
          st8(dst, u32x2{pack2v(a), pack2v(b)});
        }
      }
      BARRIER();
    } else {
      constexpr int NHW = (TYPE == T_Q) ? 4 : 2;
      const bool inval = (g == 3);
      lds_char* scr = lds + wm * 16384;
      f32x4 pp[2][2][2];
#pragma unroll
      for (int hh = 0; hh < NHW; ++hh) {
        const int head = NHW * hj + hh;
        u32x4 a0, a1;
        if (TYPE == T_KV) {
          lds_char* wp = lds + K1_WPHI + hh * 1568 + (4 * g + q4) * 56 + 8 * p4;
          a0 = tr_read2(wp, wp + 16 * 56);
          a1 = tr_read2(wp + 32, wp + 16 * 56 + 32);
        }
        f32x4 c0 = *(lds_f4*)(cstab + (wn * 8 + 2 * hh) * 16 + 4 * g);
        f32x4 c1 = *(lds_f4*)(cstab + (wn * 8 + 2 * hh + 1) * 16 + 4 * g);
#pragma unroll
        for (int mt = 0; mt < 2; ++mt) {
          u32x4 bq;
          bq[0] = pack2v(pk_fma(lo2(c0), Mr[mt], lo2(acc[mt][2 * hh]) * Rr[mt]));
          bq[1] = pack2v(pk_fma(hi2(c0), Mr[mt], hi2(acc[mt][2 * hh]) * Rr[mt]));
          bq[2] = pack2v(pk_fma(lo2(c1), Mr[mt], lo2(acc[mt][2 * hh + 1]) * Rr[mt]));
          bq[3] = pack2v(pk_fma(hi2(c1), Mr[mt], hi2(acc[mt][2 * hh + 1]) * Rr[mt]));
          bq[2] = inval ? 0u : bq[2]; bq[3] = inval ? 0u : bq[3];
          if (TYPE == T_Q) {
            const int stile = rb * 8 + wm * 2 + mt;
            st16(ws + WS_PHIQ + (size_t)(head * 128 + stile) * 1024 + lane * 16, bq);
          } else {
            f32x4 z4 = {0.f, 0.f, 0.f, 0.f};
            pp[hh & 1][mt][0] = mfma16(a0, bq, z4); pp[hh & 1][mt][1] = mfma16(a1, bq, z4);
          }
        }
      }
      if (TYPE == T_KV) {
        u32x4 vb[2][2];
        f32x4 r4[2], m4[2];
#pragma unroll
        for (int mt = 0; mt < 2; ++mt) {
          r4[mt] = *(lds_f4*)(rtab + wm * 32 + mt * 16 + 4 * g);
          m4[mt] = *(lds_f4*)(mtab + wm * 32 + mt * 16 + 4 * g);
        }
#pragma unroll
        for (int hh = 0; hh < 2; ++hh)
#pragma unroll
          for (int dt = 0; dt < 2; ++dt) {
            const int nt = 4 + 2 * hh + dt;
            float csd = cstab[(wn * 8 + nt) * 16 + l15];
            const f32x2 cc = {csd, csd};
#pragma unroll
            for (int mt = 0; mt < 2; ++mt) {
              vb[hh][dt][2 * mt] = pack2v(pk_fma(cc, lo2(m4[mt]), lo2(acc[mt][nt]) * lo2(r4[mt])));
              vb[hh][dt][2 * mt + 1] = pack2v(pk_fma(cc, hi2(m4[mt]), hi2(acc[mt][nt]) * hi2(r4[mt])));
            }
            if (dt == 1) {
              const unsigned fill = (l15 == 12) ? 0x3f803f80u : 0u;
#pragma unroll
              for (int i = 0; i < 4; ++i) vb[hh][1][i] = (l15 >= 12) ? fill : vb[hh][1][i];
            }
          }
#pragma unroll
        for (int hh = 0; hh < 2; ++hh)
#pragma unroll
          for (int mt = 0; mt < 2; ++mt) {
            u32x4 pos, neg;
            hedgehog_softmax(pp[hh][mt][0], pp[hh][mt][1], inval, pos, neg);
            lds_char* d = scr + hh * 5120 + (mt * 16 + l15) * 160 + 8 * g;
            *(lds_u2*)(d) = u32x2{pos[0], pos[1]};
            *(lds_u2*)(d + 32) = u32x2{pos[2], pos[3]};
            *(lds_u2*)(d + 64) = u32x2{neg[0], neg[1]};
            *(lds_u2*)(d + 96) = u32x2{neg[2], neg[3]};
          }
        f32x4 kv[2][4][2];
#pragma unroll
        for (int hh = 0; hh < 2; ++hh) {
          lds_char* rp = scr + hh * 5120 + (4 * g + q4) * 160 + 8 * p4;
#pragma unroll
          for (int ft = 0; ft < 4; ++ft) {
            u32x4 pa = tr_read2(rp + ft * 32, rp + 16 * 160 + ft * 32);
            f32x4 z4 = {0.f, 0.f, 0.f, 0.f};
            kv[hh][ft][0] = mfma16(pa, vb[hh][0], z4);
            kv[hh][ft][1] = mfma16(pa, vb[hh][1], z4);
          }
        }
        WAIT_LGKM0();
#pragma unroll
        for (int hh = 0; hh < 2; ++hh) {
          lds_char* q = scr + (hh == 0 ? 8192 : 0) + lane * 16;
#pragma unroll
          for (int ft = 0; ft < 4; ++ft) {
            *(lds_f4*)(q + (ft * 2 + 0) * 1024) = kv[hh][ft][0];
            *(lds_f4*)(q + (ft * 2 + 1) * 1024) = kv[hh][ft][1];
          }
        }
      }
      WAIT_LGKM0();
      BARRIER();
      if (TYPE == T_KV) {
#pragma unroll
        for (int it = 0; it < 4; ++it) {
          int pos = it * 256 + tid;
          f32x4 a = *(lds_f4*)(lds + 0 * 16384 + pos * 16);
          a += *(lds_f4*)(lds + 1 * 16384 + pos * 16);
          a += *(lds_f4*)(lds + 2 * 16384 + pos * 16);
          a += *(lds_f4*)(lds + 3 * 16384 + pos * 16);
          int head = 2 * hj + 1 - (pos >> 9);
          st16(ws + WS_KVS + ((size_t)(head * 16 + rb) * 8) * 1024 + (pos & 511) * 16, __builtin_bit_cast(u32x4, a));
        }
      }
    }
  } else {
    lds_f* st = (lds_f*)(lds + K1_ST2);
#pragma unroll
    for (int mt = 0; mt < 2; ++mt) {
      f32x4 r4 = *(lds_f4*)(rtab + wm * 32 + mt * 16 + 4 * g);
      f32x4 m4 = *(lds_f4*)(mtab + wm * 32 + mt * 16 + 4 * g);
      f32x2 sa = {0.f, 0.f}, sb = {0.f, 0.f}, qa = {0.f, 0.f}, qb = {0.f, 0.f};
#pragma unroll
      for (int nt = 0; nt < NT; ++nt) {
        float csd = cstab[(wn * 8 + nt) * 16 + l15];
        const f32x2 cc = {csd, csd};
        f32x2 a = gelu2(pk_fma(cc, lo2(m4), lo2(acc[mt][nt]) * lo2(r4)));
        f32x2 b = gelu2(pk_fma(cc, hi2(m4), hi2(acc[mt][nt]) * hi2(r4)));
        unsigned pa = pack2v(a), pb = pack2v(b);
        sa += a; sb += b; qa = pk_fma(a, a, qa); qb = pk_fma(b, b, qb);
        const int f = hj * 112 + nt * 16 + l15;
        const int s16 = rb * 8 + wm * 2 + mt;
        char* dst = ws + WS_Z2B + (size_t)((f >> 5) * 128 + s16) * 1024 + ((f & 31) + 32 * (g >> 1)) * 16 + (g & 1) * 8;
        st8(dst, u32x2{pa, pb});
      }
      f32x4 o0 = {row_sum16(sa[0]), row_sum16(qa[0]), row_sum16(sa[1]), row_sum16(qa[1])};
      f32x4 o1 = {row_sum16(sb[0]), row_sum16(qb[0]), row_sum16(sb[1]), row_sum16(qb[1])};
      if (l15 == 0) {
        *(lds_f4*)(st + (wn * 128 + wm * 32 + mt * 16 + 4 * g) * 2) = o0;
        *(lds_f4*)(st + (wn * 128 + wm * 32 + mt * 16 + 4 * g) * 2 + 4) = o1;
      }
    }
    WAIT_LGKM0();
    BARRIER();
    if (tid < 128) {
      f32x2 a = *(lds_f2*)(st + tid * 2), b = *(lds_f2*)(st + (128 + tid) * 2);
      *(f32x2*)(ws + WS_STAT + ((size_t)(cg - 10) * 2048 + row0 + tid) * 8) = a + b;
    }
  }
}

__global__ __launch_bounds__(512, 2) void k1_kernel(const __bf16* __restrict__ x, const __bf16* __restrict__ wqkv,
                                                    const __bf16* __restrict__ wphi, const __bf16* __restrict__ win,
                                                    const __bf16* __restrict__ wsp, const __bf16* __restrict__ wo,
                                                    const __bf16* __restrict__ wout, char* __restrict__ ws) {
  extern __shared__ __attribute__((aligned(16))) char smem[];
  lds_char* lds = (lds_char*)smem;
  RSTAMP(0, 14);
  const int b = blockIdx.x;
  const int xcd = b & 7, idx = b >> 3;
  const int tid = threadIdx.x, lane = tid & 63;
  if (idx >= 28) {
    const int e = xcd * 4 + (idx - 28);
    unsigned acc = 0;
    const u32x4* p = (const u32x4*)wsp + (size_t)e * 16384 + tid;
#pragma unroll 8
    for (int i = 0; i < 32; ++i) { u32x4 v = p[i * 512]; acc ^= v[0] ^ v[3]; }
    const u32x4* q = (const u32x4*)wout + e * 1568 + tid;
#pragma unroll
    for (int i = 0; i < 4; ++i) if (i * 512 + tid < 1568) { u32x4 v = q[i * 512]; acc ^= v[0] ^ v[3]; }
    const u32x4* r = (const u32x4*)wo + e * 784 + tid;
#pragma unroll
    for (int i = 0; i < 2; ++i) if (i * 512 + tid < 784) { u32x4 v = r[i * 512]; acc ^= v[0] ^ v[3]; }
    asm volatile("" :: "v"(acc));
    return;
  }
  const int rb = 2 * xcd + idx / 14, cg = idx % 14;
  const int wave = __builtin_amdgcn_readfirstlane(tid >> 6);
  const unsigned ldsb = (unsigned)(size_t)lds;
  int ht[2], hj[2];
  k1_half(cg, 0, ht[0], hj[0]);
  k1_half(cg, 1, ht[1], hj[1]);
  unsigned goff[4], xoff[2];
  const char* Wh[2]; int ldw[2];
#pragma unroll
  for (int h = 0; h < 2; ++h) {
    const bool qkv = (ht[h] == T_Q || ht[h] == T_KV);
    Wh[h] = uni_ptr(qkv ? (const char*)wqkv : (const char*)win);
    ldw[h] = __builtin_amdgcn_readfirstlane(qkv ? 2688 : 3584);
#pragma unroll
    for (int i = 0; i < 2; ++i) {
      int row = (i * 8 + wave) * 4 + (lane >> 4);
      int sw = (row & 3) | (((row >> 3) & 1) << 2);
      int c = (lane & 15) ^ (sw << 1);
      int colb;
      if (ht[h] == T_Q) colb = 224 * hj[h] + 16 * min(c, 14);
      else if (ht[h] == T_KV) colb = (c < 7) ? (896 + 112 * hj[h] + 16 * c) : (c < 14 ? (1792 + 112 * hj[h] + 16 * (c - 7)) : (896 + 112 * hj[h]));
      else colb = (ht[h] == T_Z2 ? 1792 : 0) + 224 * hj[h] + 16 * (c < 14 ? c : 0);
      goff[h * 2 + i] = row * ldw[h] + colb;
    }
  }
#pragma unroll
  for (int i = 0; i < 2; ++i) {
    int row = (i * 8 + wave) * 8 + (lane >> 3);
    int c = (lane & 7) ^ ((row >> 1) & 7);
    xoff[i] = (rb * 128 + row) * 896 + c * 16;
  }
#pragma unroll
  for (int chunk = 0; chunk < 3; ++chunk) {
    const unsigned dx = ldsb + K1_XOFF + chunk * K1_XST + wave * 1024;
    const unsigned dw = ldsb + chunk * K1_WST + wave * 1024;
    const char* xb = (const char*)x + chunk * 128;
    glds16(xb, xoff[0], dx);
    glds16(xb, xoff[1], dx + 8192);
#pragma unroll
    for (int i = 0; i < 4; ++i) glds16(Wh[i >> 1] + (size_t)chunk * 64 * ldw[i >> 1], goff[i], dw + i * 8192);
  }
  const int my = (wave >> 2) ? ht[1] : ht[0], mj = (wave >> 2) ? hj[1] : hj[0];
  if (my == T_Q) k1_wave<T_Q>(x, Wh[0], Wh[1], ldw[0], ldw[1], goff, xoff, wphi, ws, lds, rb, cg, mj);
  else if (my == T_KV) k1_wave<T_KV>(x, Wh[0], Wh[1], ldw[0], ldw[1], goff, xoff, wphi, ws, lds, rb, cg, mj);
  else if (my == T_Z1) k1_wave<T_Z1>(x, Wh[0], Wh[1], ldw[0], ldw[1], goff, xoff, wphi, ws, lds, rb, cg, mj);
  else k1_wave<T_Z2>(x, Wh[0], Wh[1], ldw[0], ldw[1], goff, xoff, wphi, ws, lds, rb, cg, mj);
  STAMP(0, 11);
  RSTAMP(0, 15);
}

constexpr int K2_WB = 0;
constexpr int K2_RT = 32768;
constexpr int K2_MT = K2_RT + 8192;
constexpr int K2_CT = K2_MT + 4096;
constexpr int K2_LDS = K2_CT + 512;

__global__ __launch_bounds__(768, 3) void k2_kernel(const __bf16* __restrict__ wsp, const __bf16* __restrict__ bsp,
                                                    const __bf16* __restrict__ wphi, char* __restrict__ ws) {
  extern __shared__ __attribute__((aligned(16))) char smem[];
  lds_char* lds = (lds_char*)smem;
  const int tid = threadIdx.x, lane = tid & 63;
  const int wave = __builtin_amdgcn_readfirstlane(tid >> 6);
  const int b = blockIdx.x, xcd = b & 7, idx = b >> 3;
  RSTAMP(1, 14);
  STAMP(1, 0);
  if (idx >= 28) {
    if (wave >= 8) return;
    const int e = xcd * 4 + (idx - 28), head = e >> 1, half = e & 1;
    const int g = lane >> 4, l15 = lane & 15, q4 = (lane >> 2) & 3, p4 = lane & 3;
    u32x4 qf[8];
    const char* pbase = ws + WS_PHIQ + (size_t)(head * 128 + half * 64 + wave * 8) * 1024 + lane * 16;
#pragma unroll
    for (int i = 0; i < 8; ++i) qf[i] = *(const u32x4*)(pbase + i * 1024);
    if (tid < 128) {
      const unsigned o = min(head * 1568 + tid * 16, 25088 - 16);
      *(lds_u4*)(lds + 8192 + tid * 16) = *(const u32x4*)((const char*)wphi + o);
    }
    {
      const char* kb = ws + WS_KVS + ((size_t)(head * 16) * 8 + wave) * 1024 + lane * 16;
      f32x4 a = {0.f, 0.f, 0.f, 0.f};
#pragma unroll
      for (int r = 0; r < 16; ++r) a += *(const f32x4*)(kb + (size_t)r * 8192);
      *(lds_u2*)(lds + wave * 512 + lane * 8) = u32x2{pack2(a[0], a[1]), pack2(a[2], a[3])};
    }
    __syncthreads();
    u32x4 af[2][2];
#pragma unroll
    for (int sg = 0; sg < 2; ++sg)
#pragma unroll
      for (int dt = 0; dt < 2; ++dt) {
        u32x2 lo = *(lds_u2*)(lds + ((2 * sg) * 2 + dt) * 512 + lane * 8);
        u32x2 hi = *(lds_u2*)(lds + ((2 * sg + 1) * 2 + dt) * 512 + lane * 8);
        af[sg][dt] = u32x4{lo[0], lo[1], hi[0], hi[1]};
      }
    lds_char* wp = lds + 8192 + (4 * g + q4) * 56 + 8 * p4;
    const u32x4 a0 = tr_read2(wp, wp + 16 * 56), a1 = tr_read2(wp + 32, wp + 16 * 56 + 32);
    const bool inval = (g == 3);
#pragma unroll
    for (int i = 0; i < 8; ++i) {
      f32x4 z4 = {0.f, 0.f, 0.f, 0.f};
      f32x4 p0 = mfma16(a0, qf[i], z4), p1 = mfma16(a1, qf[i], z4);
      u32x4 pos, neg;
      hedgehog_softmax(p0, p1, inval, pos, neg);
      f32x4 n0 = mfma16(af[0][0], pos, z4); n0 = mfma16(af[1][0], neg, n0);
      f32x4 n1 = mfma16(af[0][1], pos, z4); n1 = mfma16(af[1][1], neg, n1);
      float den = __shfl(n1[0], 48 + l15);
      den = rbf(rbf(den) + 1e-6f);
      float rd = 1.f / den;
      const int s = half * 1024 + (wave * 8 + i) * 16 + l15;
#pragma unroll
      for (int dt = 0; dt < 2; ++dt) {
        const f32x4& n = dt ? n1 : n0;
        u32x2 o = {pack2(rbf(n[0]) * rd, rbf(n[1]) * rd), pack2(rbf(n[2]) * rd, rbf(n[3]) * rd)};
        const int k = 28 * head + 16 * dt + 4 * g;
        const int kl = k & 15, hh = (kl >> 2) & 1, j0 = (kl >> 3) * 4;
        char* dst = ws + WS_HO + (size_t)((s >> 5) * 84 + (k >> 4)) * 1024 + ((s & 31) + 32 * hh) * 16 + j0 * 2;
        if (!(dt == 1 && g == 3)) st8(dst, o);
      }
    }
    STAMP(1, 4);
    RSTAMP(1, 15);
    return;
  }

  const int rb = 4 * xcd + idx / 7, ct = idx % 7;
  constexpr int NK = 16;
  const int koff = (ct * 2 + (ct > 3 ? 1 : 0)) & 15;
#define KROT(k) ((((k) + koff) & 15))
  auto stat_load = [&](int row, f32x2 (&a)[4]) {
#pragma unroll
    for (int gq = 0; gq < 4; ++gq) a[gq] = *(const f32x2*)(ws + WS_STAT + ((size_t)gq * 2048 + row) * 8);
  };
  auto stat_put = [&](int row, const f32x2 (&a)[4]) {
    f32x2 t = (a[0] + a[1]) + (a[2] + a[3]);
    float mean = t[0] * (1.f / 896.f);
    float var = fmaxf(t[1] * (1.f / 896.f) - mean * mean, 0.f);
    float vb = rbf(var);
    float sd = rbf(__builtin_amdgcn_sqrtf(rbf(vb + 1e-5f)));
    *(lds_f*)(lds + K2_RT + row * 4) = frcp(sd);
    *(__attribute__((address_space(3))) unsigned short*)(lds + K2_MT + row * 2) = (unsigned short)pack2(mean, 0.f);
  };
  const int srow = KROT(tid >> 7) * 128 + (tid & 127);
  f32x2 sa0[4];
  if (wave >= 4) {
    __builtin_amdgcn_s_setprio(2);
    const int pt = tid - 256, t = pt >> 4, cp = pt & 15;
    const int g = lane >> 4, l15 = lane & 15;
    const char* src = (const char*)wsp + (size_t)(rb * 64 + t) * 4096 + cp * 16;
    constexpr int PD = 4;
    u32x4 wr[PD][2];
#define WLOAD(dst, kk) do { _Pragma("unroll") for (int j = 0; j < 2; ++j) \
      dst[j] = *(const u32x4*)(src + KROT(kk) * 256 + j * (32 * 4096)); } while (0)
    WLOAD(wr[0], 0);
    if (tid < 512) {
      stat_load(srow, sa0);
      __builtin_amdgcn_sched_barrier(0);
      stat_put(srow, sa0);
    }
    const int wa = t * 256 + ((cp ^ (t & 15)) << 4);
    const int ta = cp * 32;
    const int pw = wave - 4;
    const int crow = (pw >> 1) * 16 + l15;
    const int ca = (crow * 256 + ((g ^ l15) << 4)) ^ ((pw & 1) << 7);
    f32x4 cacc = {0.f, 0.f, 0.f, 0.f};
    WAIT_LGKM0(); BARRIER();
    f32x4 rr[2][2];
    rr[0][0] = *(lds_f4*)(lds + K2_RT + KROT(0) * 512 + ta); rr[0][1] = *(lds_f4*)(lds + K2_RT + KROT(0) * 512 + ta + 16);
    WLOAD(wr[1], 1);
    f32x2 sb[3][4];
    __builtin_amdgcn_sched_barrier(0);
#pragma unroll
    for (int k = 0; k <= NK; ++k) {
      if (k == 1) {
#pragma unroll
        for (int i = 0; i < 3; ++i) stat_load(KROT(4 + 4 * i + (pt >> 7)) * 128 + (pt & 127), sb[i]);
      }
      lds_char* wb = lds + K2_WB + (k & 1) * 16384;
      lds_char* pb = lds + K2_WB + ((k + 1) & 1) * 16384;
      u32x4 mf[2], wf[2];
#pragma unroll
      for (int j = 0; j < 2; ++j) {
        if (k > 0) {
          mf[j] = *(lds_u4*)(lds + K2_MT + (KROT(k - 1) * 128 + ((pw & 1) * 2 + j) * 32 + g * 8) * 2);
          wf[j] = *(lds_u4*)(pb + (ca ^ (j << 6)));
        }
      }
      if (k + 1 < NK) {
        rr[(k + 1) & 1][0] = *(lds_f4*)(lds + K2_RT + KROT(k + 1) * 512 + ta);
        rr[(k + 1) & 1][1] = *(lds_f4*)(lds + K2_RT + KROT(k + 1) * 512 + ta + 16);
      }
      __builtin_amdgcn_sched_barrier(0);
#pragma unroll
      for (int j = 0; j < 2; ++j) {
        if (k < NK) {
          const f32x4 r0 = rr[k & 1][0], r1 = rr[k & 1][1];
          u32x4 w = wr[k % PD][j];
          if (k + PD < NK) wr[k % PD][j] = *(const u32x4*)(src + KROT(k + PD) * 256 + j * (32 * 4096));
          u32x4 o;
          o[0] = pack2(smul(bf_lo(w[0]), r0[0]), smul(bf_hi(w[0]), r0[1]));
          o[1] = pack2(smul(bf_lo(w[1]), r0[2]), smul(bf_hi(w[1]), r0[3]));
          o[2] = pack2(smul(bf_lo(w[2]), r1[0]), smul(bf_hi(w[2]), r1[1]));
          o[3] = pack2(smul(bf_lo(w[3]), r1[2]), smul(bf_hi(w[3]), r1[3]));
          *(lds_u4*)(wb + wa + j * 8192) = o;
        }
        __builtin_amdgcn_sched_barrier(0);
        if (k > 0) cacc = mfma16(mf[j], wf[j], cacc);
      }
      if (k == 0) { WLOAD(wr[2], 2); WLOAD(wr[3], 3); }
      if (k == 2) {
#pragma unroll
        for (int i = 0; i < 3; ++i) stat_put(KROT(4 + 4 * i + (pt >> 7)) * 128 + (pt & 127), sb[i]);
      }
      if (k == NK) {
        if (g == 0) *(lds_f*)(lds + K2_CT + ((pw & 1) * 64 + crow) * 4) = cacc[0];
      }
      WAIT_LGKM0();
      BARRIER();
    }
  } else {
    const int F = 4 * ct + wave, l31 = lane & 31, hh = lane >> 5;
    const char* zsrc = ws + WS_Z2B + (size_t)F * 128 * 1024 + lane * 16;
    constexpr int ZD = 16;
    u32x4 zf[ZD];
#pragma unroll
    for (int i = 0; i < 8; ++i) zf[i] = *(const u32x4*)(zsrc + (KROT(i >> 3) * 8 + (i & 7)) * 1024);
    stat_load(srow, sa0);
    __builtin_amdgcn_sched_barrier(0);
    stat_put(srow, sa0);
    WAIT_LGKM0(); BARRIER();
    STAMP(1, 1);
    const int ra = l31 * 256 + ((hh ^ (l31 & 15)) << 4);
    f32x16 acc[2];
#pragma unroll
    for (int i = 0; i < 16; ++i) { acc[0][i] = 0.f; acc[1][i] = 0.f; }
    u32x2 z1v[2][4];
    float bb[2];
    BARRIER();
    STAMP(1, 2);
    int rk[8];
#pragma unroll
    for (int kk = 0; kk < 8; ++kk) { rk[kk] = ra ^ (kk << 5); }
    constexpr int LD = 3;
    u32x4 wq[LD + 1][2];
#pragma unroll
    for (int i = 0; i < LD; ++i) { wq[i][0] = *(lds_u4*)(lds + rk[i]); wq[i][1] = *(lds_u4*)(lds + rk[i] + 8192); }
#pragma unroll
    for (int k = 0; k < NK; ++k) {
#pragma unroll
      for (int kk = 0; kk < 8; ++kk) {
        const int ks = k * 8 + kk;
        if (kk == 8 - LD && k + 1 < NK) { WAIT_LGKM0(); BARRIER(); }
        if (ks + LD < 128) {
          const int nb = (((ks + LD) >> 3) & 1) * 16384;
          wq[(ks + LD) % (LD + 1)][0] = *(lds_u4*)(lds + rk[(kk + LD) & 7] + nb);
          wq[(ks + LD) % (LD + 1)][1] = *(lds_u4*)(lds + rk[(kk + LD) & 7] + nb + 8192);
        }
        const u32x4 w0 = wq[ks % (LD + 1)][0], w1 = wq[ks % (LD + 1)][1];
        if (ks == 108) {
#pragma unroll
          for (int tt = 0; tt < 2; ++tt) {
            const int t = rb * 64 + tt * 32 + l31;
            bb[tt] = bf_lo((unsigned)*(const unsigned short*)(bsp + t));
            const char* z1p = ws + WS_Z1B + (size_t)((t >> 5) * 28 + F) * 2048 + lane * 8;
#pragma unroll
            for (int R = 0; R < 4; ++R) z1v[tt][R] = *(const u32x2*)(z1p + R * 512);
          }
        }
        u32x4 z = zf[ks % ZD];
#define ZLOAD(i) zf[(i) % ZD] = *(const u32x4*)(zsrc + (KROT((i) >> 3) * 8 + ((i) & 7)) * 1024)
        if (ks < ZD - 8) { ZLOAD(8 + 2 * ks); ZLOAD(9 + 2 * ks); }
        else if (ks + ZD < 128) ZLOAD(ks + ZD);
        acc[0] = mfma32(z, w0, acc[0]);
        acc[1] = mfma32(z, w1, acc[1]);
        __builtin_amdgcn_sched_barrier(0);
      }
    }
    WAIT_LGKM0();
    BARRIER();
    STAMP(1, 3);
#pragma unroll
    for (int tt = 0; tt < 2; ++tt) {
      const int t = rb * 64 + tt * 32 + l31;
      const float c = *(lds_f*)(lds + K2_CT + (tt * 32 + l31) * 4) + *(lds_f*)(lds + K2_CT + (64 + tt * 32 + l31) * 4);
      const f32x2 cc = {c, c}, b2 = {bb[tt], bb[tt]};
      unsigned hp[8];
#pragma unroll
      for (int R = 0; R < 4; ++R) {
        f32x2 g0 = unpack2(pack2v(f32x2{acc[tt][4 * R + 0], acc[tt][4 * R + 1]} - cc));
        f32x2 g1 = unpack2(pack2v(f32x2{acc[tt][4 * R + 2], acc[tt][4 * R + 3]} - cc));
        g0 = unpack2(pack2v(g0 + b2)); g1 = unpack2(pack2v(g1 + b2));
        hp[2 * R] = pack2v(unpack2(z1v[tt][R][0]) * g0); hp[2 * R + 1] = pack2v(unpack2(z1v[tt][R][1]) * g1);
      }
      char* dst = ws + WS_HO + (size_t)((t >> 5) * 84 + 28 + 2 * F) * 1024 + lane * 16;
      st16(dst, u32x4{hp[0], hp[1], hp[2], hp[3]});
      st16(dst + 1024, u32x4{hp[4], hp[5], hp[6], hp[7]});
    }
    STAMP(1, 4);
    RSTAMP(1, 15);
  }
}

constexpr int K3_STG = 16384;
constexpr int K3_NB = 5;
constexpr int K3_LDS = K3_STG * K3_NB;

__global__ __launch_bounds__(256, 2) void k3_kernel(const __bf16* __restrict__ x, const __bf16* __restrict__ wo,
                                                    const __bf16* __restrict__ wout, char* __restrict__ ws,
                                                    __bf16* __restrict__ out) {
  extern __shared__ __attribute__((aligned(16))) char smem[];
  lds_char* lds = (lds_char*)smem;
  const unsigned ldsb = (unsigned)(size_t)lds;
  const int tid = threadIdx.x, lane = tid & 63;
  const int wave = __builtin_amdgcn_readfirstlane(tid >> 6);
  const int b = blockIdx.x, xcd = b & 7, idx = b >> 3;
  const int rb = 4 * xcd + idx / 7, ct = idx % 7;
  const int wt = wave & 1, wn = wave >> 1;
  const int l31 = lane & 31, hh = lane >> 5, gi = lane >> 4, q4 = (lane >> 2) & 3, p4 = lane & 3;
  RSTAMP(2, 14);
  STAMP(2, 0);

  unsigned goff[2];
#pragma unroll
  for (int i = 0; i < 2; ++i) {
    int row = (i * 4 + wave) * 8 + (lane >> 3);
    int sw = ((row & 3) << 1) ^ (((row >> 3) & 1));
    int c = (lane & 7) ^ sw;
    goff[i] = row * 896 + ct * 128 + c * 16;
  }
  const char* abase = ws + WS_HO + (size_t)(rb * 2) * 84 * 1024;
  const unsigned aoff = wave * 1024 + lane * 16;
  auto stage = [&](int chunk) {
    const char* base = (chunk < 7) ? ((const char*)wo + (size_t)chunk * 64 * 896) : ((const char*)wout + (size_t)(chunk - 7) * 64 * 896);
    const unsigned dst = ldsb + (chunk % K3_NB) * K3_STG + wave * 1024;
    glds16(abase + chunk * 4096, aoff, dst);
    glds16(abase + chunk * 4096 + 84 * 1024, aoff, dst + 4096);
    glds16(base, goff[0], dst + 8192);
    glds16(base, goff[1], dst + 8192 + 4096);
  };
  const int t = rb * 64 + wt * 32 + l31;
  u32x2 xv[4];
  {
    const unsigned xo = (unsigned)(t * D_ + ct * 64 + wn * 32 + 4 * hh) * 2;
    asm volatile("global_load_dwordx2 %0, %4, %5\n\tglobal_load_dwordx2 %1, %4, %5 offset:16\n\t"
                 "global_load_dwordx2 %2, %4, %5 offset:32\n\tglobal_load_dwordx2 %3, %4, %5 offset:48"
                 : "=&v"(xv[0]), "=&v"(xv[1]), "=&v"(xv[2]), "=&v"(xv[3]) : "v"(xo), "s"(x) : "memory");
  }
  stage(0); stage(1); stage(2); stage(3);

  int baddr;
  {
    int row = 4 * (gi >> 1) + q4;
    int byte = 2 * (wn * 32 + (gi & 1) * 16 + 4 * p4);
    int sw = ((row & 3) << 1);
    baddr = 8192 + row * 128 + (((byte >> 4) ^ sw) << 4) + (byte & 15);
  }
  const int baddr2 = (baddr ^ 16) + 8 * 128;
  const int aaddr = wt * 4096 + lane * 16;

  f32x16 acc;
#pragma unroll
  for (int i = 0; i < 16; ++i) acc[i] = 0.f;
  unsigned t1[8];
  asm volatile("s_waitcnt vmcnt(12)" : "+v"(xv[0]), "+v"(xv[1]), "+v"(xv[2]), "+v"(xv[3]) :: "memory");
  BARRIER();
  STAMP(2, 1);
  u32x4 wf = tr_read2(lds + baddr, lds + baddr2), af = *(lds_u4*)(lds + aaddr);
#pragma unroll
  for (int c = 0; c < 21; ++c) {
#pragma unroll
    for (int kk = 0; kk < 4; ++kk) {
      u32x4 wn_ = wf, an_ = af;
      if (kk == 3 && c + 1 < 21) {
        if (c + 3 < 21) WAIT_VM(8); else if (c + 2 < 21) WAIT_VM(4); else WAIT_VM(0);
        WAIT_LGKM0();
        BARRIER();
        if (c + 4 < 21) stage(c + 4);
      }
      if (c * 4 + kk + 1 < 84) {
        const int cn = (kk == 3) ? c + 1 : c, kn = (kk + 1) & 3;
        lds_char* bufp = lds + (cn % K3_NB) * K3_STG;
        wn_ = tr_read2(bufp + baddr + kn * 2048, bufp + baddr2 + kn * 2048);
        an_ = *(lds_u4*)(bufp + aaddr + kn * 1024);
      }
      acc = mfma32(wf, af, acc);
      wf = wn_; af = an_;
      __builtin_amdgcn_sched_barrier(0);
    }
    if (c == 6) {
#pragma unroll
      for (int R = 0; R < 4; ++R) {
        f32x2 a0 = unpack2(pack2v(f32x2{acc[4 * R + 0], acc[4 * R + 1]}));
        f32x2 a1 = unpack2(pack2v(f32x2{acc[4 * R + 2], acc[4 * R + 3]}));
        t1[2 * R] = pack2v(unpack2(xv[R][0]) + a0); t1[2 * R + 1] = pack2v(unpack2(xv[R][1]) + a1);
      }
#pragma unroll
      for (int i = 0; i < 16; ++i) acc[i] = 0.f;
    }
  }
  STAMP(2, 2);
  __bf16* orow = out + (size_t)t * D_ + ct * 64 + wn * 32 + 4 * hh;
#pragma unroll
  for (int R = 0; R < 4; ++R) {
    f32x2 a0 = unpack2(pack2v(f32x2{acc[4 * R + 0], acc[4 * R + 1]}));
    f32x2 a1 = unpack2(pack2v(f32x2{acc[4 * R + 2], acc[4 * R + 3]}));
    *(u32x2*)(orow + 8 * R) = u32x2{pack2v(unpack2(t1[2 * R]) + a0), pack2v(unpack2(t1[2 * R + 1]) + a1)};
  }
  STAMP(2, 3);
  RSTAMP(2, 15);
}

extern "C" void kernel_launch(void* const* d_in, const int* in_sizes, int n_in, void* d_out, int out_size,
                              void* d_ws, size_t ws_size, hipStream_t stream) {
  const __bf16* x = (const __bf16*)d_in[0];
  const __bf16* wqkv = (const __bf16*)d_in[1];
  const __bf16* wphi = (const __bf16*)d_in[2];
  const __bf16* wo = (const __bf16*)d_in[3];
  const __bf16* win = (const __bf16*)d_in[5];
  const __bf16* wsp = (const __bf16*)d_in[6];
  const __bf16* bsp = (const __bf16*)d_in[7];
  const __bf16* wout = (const __bf16*)d_in[8];
  char* ws = (char*)d_ws;
  (void)hipFuncSetAttribute((const void*)k1_kernel, hipFuncAttributeMaxDynamicSharedMemorySize, K1_LDS);
  (void)hipFuncSetAttribute((const void*)k3_kernel, hipFuncAttributeMaxDynamicSharedMemorySize, K3_LDS);
  k1_kernel<<<256, 512, K1_LDS, stream>>>(x, wqkv, wphi, win, wsp, wo, wout, ws);
  k2_kernel<<<256, 768, K2_LDS, stream>>>(wsp, bsp, wphi, ws);
  k3_kernel<<<224, 256, K3_LDS, stream>>>(x, wo, wout, ws, (__bf16*)d_out);
}
```

```cpp
#include <hip/hip_runtime.h>
#include <hip/hip_bf16.h>
#include <stdint.h>

#define DI __device__ __forceinline__
typedef __attribute__((ext_vector_type(8))) __bf16 bf16x8;
typedef __attribute__((ext_vector_type(2))) __bf16 bf16x2;
typedef __attribute__((ext_vector_type(4))) short s16x4;
typedef __attribute__((ext_vector_type(4))) float f32x4;
typedef __attribute__((ext_vector_type(2))) float f32x2;
typedef __attribute__((ext_vector_type(16))) float f32x16;
typedef __attribute__((ext_vector_type(4))) unsigned u32x4;
typedef __attribute__((ext_vector_type(2))) unsigned u32x2;
typedef __attribute__((address_space(3))) char lds_char;
typedef __attribute__((address_space(3))) s16x4 lds_s16x4;

constexpr int S_ = 2048, D_ = 448;

constexpr size_t WS_PHIQ = 0;
constexpr size_t WS_KVS = 4u << 20;
constexpr size_t WS_Z1B = 6u << 20;
constexpr size_t WS_Z2B = 10u << 20;
constexpr size_t WS_STAT = 14u << 20;
constexpr size_t WS_HO = 15u << 20;

DI unsigned pack2(float a, float b) {
  f32x2 v = {a, b};
  return __builtin_bit_cast(unsigned, __builtin_convertvector(v, bf16x2));
}
DI float bf_lo(unsigned u) { return __uint_as_float(u << 16); }
DI float bf_hi(unsigned u) { return __uint_as_float(u & 0xffff0000u); }
DI float rbf(float x) { return bf_lo(pack2(x, 0.f)); }
DI f32x4 mfma16(u32x4 a, u32x4 b, f32x4 c) {
  return __builtin_amdgcn_mfma_f32_16x16x32_bf16(__builtin_bit_cast(bf16x8, a), __builtin_bit_cast(bf16x8, b), c, 0, 0, 0);
}
DI f32x16 mfma32(u32x4 a, u32x4 b, f32x16 c) {
  return __builtin_amdgcn_mfma_f32_32x32x16_bf16(__builtin_bit_cast(bf16x8, a), __builtin_bit_cast(bf16x8, b), c, 0, 0, 0);
}
DI u32x2 tr_read(lds_char* p) {
  s16x4 r = __builtin_amdgcn_ds_read_tr16_b64_v4i16((lds_s16x4*)p);
  return __builtin_bit_cast(u32x2, r);
}
DI u32x4 tr_read2(lds_char* p0, lds_char* p1) {
  u32x2 a = tr_read(p0), b = tr_read(p1);
  u32x4 r = {a[0], a[1], b[0], b[1]};
  return r;
}
DI float frcp(float x) { return __builtin_amdgcn_rcpf(x); }
DI const char* uni_ptr(const char* p) {
  unsigned long long v = (unsigned long long)p;
  unsigned lo = __builtin_amdgcn_readfirstlane((unsigned)v), hi = __builtin_amdgcn_readfirstlane((unsigned)(v >> 32));
  return (const char*)(((unsigned long long)hi << 32) | lo);
}
DI void glds16(const void* sbase, unsigned voff, unsigned lds_dst) {
  asm volatile("s_mov_b32 m0, %2\n\ts_nop 3\n\tglobal_load_lds_dwordx4 %0, %1"
               :: "v"(voff), "s"(sbase), "s"(lds_dst) : "memory");
}
DI void st16(void* p, u32x4 v) {
  asm volatile("global_store_dwordx4 %0, %1, off sc0 sc1\n\ts_nop 1" :: "v"(p), "v"(v) : "memory");
}
DI void st8(void* p, u32x2 v) {
  asm volatile("global_store_dwordx2 %0, %1, off sc0 sc1\n\ts_nop 1" :: "v"(p), "v"(v) : "memory");
}
#define WAIT_VMI(n) asm volatile("s_waitcnt vmcnt(%0)" :: "i"(n) : "memory")
#define WAIT_VM(n) asm volatile("s_waitcnt vmcnt(" #n ")" ::: "memory")
#define WAIT_LGKM0() asm volatile("s_waitcnt lgkmcnt(0)" ::: "memory")
#define BARRIER() __builtin_amdgcn_s_barrier()

constexpr int K1_WST = 32768;
constexpr int K1_XOFF = 98304;
constexpr int K1_XST = 16384;
constexpr int K1_WPHI = 131072;
constexpr int K1_CS = K1_WPHI + 13312;
constexpr int K1_RT = K1_CS + 1024;
constexpr int K1_MT = K1_RT + 512;
constexpr int K1_ST2 = K1_MT + 512;
constexpr int K1_LDS = 147456 + 2048;

enum { T_Q = 0, T_KV = 1, T_Z1 = 2, T_Z2 = 3 };

typedef __attribute__((address_space(3))) float lds_f;
typedef __attribute__((address_space(3))) f32x4 lds_f4;
typedef __attribute__((address_space(3))) f32x2 lds_f2;
typedef __attribute__((address_space(3))) u32x4 lds_u4;
typedef __attribute__((address_space(3))) u32x2 lds_u2;

DI float amax(float a, float b) { float r; asm("v_max_f32 %0, %1, %2" : "=v"(r) : "v"(a), "v"(b)); return r; }
DI float amin(float a, float b) { float r; asm("v_min_f32 %0, %1, %2" : "=v"(r) : "v"(a), "v"(b)); return r; }
#define GRP_RED(x, OP) do { \
    auto r_ = __builtin_amdgcn_permlane16_swap(__float_as_uint(x), __float_as_uint(x), false, false); \
    x = OP(__uint_as_float(r_[0]), __uint_as_float(r_[1])); \
    auto q_ = __builtin_amdgcn_permlane32_swap(__float_as_uint(x), __float_as_uint(x), false, false); \
    x = OP(__uint_as_float(q_[0]), __uint_as_float(q_[1])); } while (0)
DI float aadd(float a, float b) { return a + b; }
DI float smul(float a, float b) { float r; asm("v_mul_f32 %0, %1, %2" : "=v"(r) : "v"(a), "v"(b)); return r; }
DI f32x2 pk_fma(f32x2 a, f32x2 b, f32x2 c) { return __builtin_elementwise_fma(a, b, c); }
DI f32x2 lo2(const f32x4& v) { return f32x2{v[0], v[1]}; }
DI f32x2 hi2(const f32x4& v) { return f32x2{v[2], v[3]}; }
DI unsigned pack2v(f32x2 v) { return __builtin_bit_cast(unsigned, __builtin_convertvector(v, bf16x2)); }
DI f32x2 unpack2(unsigned u) { return f32x2{bf_lo(u), bf_hi(u)}; }
DI f32x2 gelu2(f32x2 v) {
  const f32x2 c1 = {-2.302208198f, -2.302208198f};
  const f32x2 c2 = {-0.1029432397f, -0.1029432397f};
  f32x2 u = v * pk_fma(v * v, c2, c1);
  f32x2 d = f32x2{__builtin_amdgcn_exp2f(u[0]), __builtin_amdgcn_exp2f(u[1])} + f32x2{1.f, 1.f};
  return v * f32x2{frcp(d[0]), frcp(d[1])};
}
DI float row_sum16(float x) {
  x += __builtin_bit_cast(float, __builtin_amdgcn_update_dpp(0, __builtin_bit_cast(int, x), 0x128, 0xf, 0xf, false));
  x += __builtin_bit_cast(float, __builtin_amdgcn_update_dpp(0, __builtin_bit_cast(int, x), 0x124, 0xf, 0xf, false));
  x += __builtin_bit_cast(float, __builtin_amdgcn_update_dpp(0, __builtin_bit_cast(int, x), 0x122, 0xf, 0xf, false));
  x += __builtin_bit_cast(float, __builtin_amdgcn_update_dpp(0, __builtin_bit_cast(int, x), 0x121, 0xf, 0xf, false));
  return x;
}

DI void hedgehog_softmax(const f32x4& p0, const f32x4& p1, bool inval, u32x4& pos, u32x4& neg) {
  f32x2 v[4];
  v[0] = unpack2(pack2v(lo2(p0))); v[1] = unpack2(pack2v(hi2(p0)));
  v[2] = unpack2(pack2v(lo2(p1))); v[3] = unpack2(pack2v(hi2(p1)));
  f32x2 w2 = inval ? v[0] : v[2], w3 = inval ? v[1] : v[3];
  float mx = __builtin_fmaxf(__builtin_fmaxf(__builtin_fmaxf(v[0][0], v[0][1]), __builtin_fmaxf(v[1][0], v[1][1])),
                             __builtin_fmaxf(__builtin_fmaxf(w2[0], w2[1]), __builtin_fmaxf(w3[0], w3[1])));
  float mn = __builtin_fminf(__builtin_fminf(__builtin_fminf(v[0][0], v[0][1]), __builtin_fminf(v[1][0], v[1][1])),
                             __builtin_fminf(__builtin_fminf(w2[0], w2[1]), __builtin_fminf(w3[0], w3[1])));
  GRP_RED(mx, amax);
  GRP_RED(mn, amin);
  const float L = 1.4426950408889634f;
  const f32x2 L2 = {L, L}, nL2 = {-L, -L};
  const f32x2 bx = {-mx * L, -mx * L}, bn = {mn * L, mn * L};
  f32x2 e1[4], e2[4];
#pragma unroll
  for (int i = 0; i < 4; ++i) {
    f32x2 a = pk_fma(v[i], L2, bx), b = pk_fma(v[i], nL2, bn);
    e1[i] = f32x2{__builtin_amdgcn_exp2f(a[0]), __builtin_amdgcn_exp2f(a[1])};
    e2[i] = f32x2{__builtin_amdgcn_exp2f(b[0]), __builtin_amdgcn_exp2f(b[1])};
  }
  const f32x2 zz = {0.f, 0.f};
  e1[2] = inval ? zz : e1[2]; e1[3] = inval ? zz : e1[3];
  e2[2] = inval ? zz : e2[2]; e2[3] = inval ? zz : e2[3];
  f32x2 t1 = (e1[0] + e1[1]) + (e1[2] + e1[3]), t2 = (e2[0] + e2[1]) + (e2[2] + e2[3]);
  float s1 = t1[0] + t1[1], s2 = t2[0] + t2[1];
  GRP_RED(s1, aadd);
  GRP_RED(s2, aadd);
  float r1 = frcp(s1), r2 = frcp(s2);
  const f32x2 R1 = {r1, r1}, R2 = {r2, r2};
#pragma unroll
  for (int i = 0; i < 4; ++i) { pos[i] = pack2v(e1[i] * R1); neg[i] = pack2v(e2[i] * R2); }
}

DI void k1_half(int cg, int wn, int& ht, int& j) {
  const int lo = (wn == 0) ? T_KV : (cg < 4 ? T_Q : T_Z1);
  ht = cg < 8 ? lo : (cg < 10 ? T_Z1 : T_Z2);
  const int jlo = (wn == 0 || cg < 4) ? cg : cg - 4;
  j = cg < 8 ? jlo : (cg < 10 ? 2 * cg - 12 + wn : 2 * cg - 20 + wn);
}

template <int TYPE>
DI void k1_wave(const __bf16* __restrict__ x, const char* Wh0, const char* Wh1, int ldw0, int ldw1,
                const unsigned (&goff)[4], const unsigned (&xoff)[2], const __bf16* __restrict__ wphi,
                char* __restrict__ ws, lds_char* lds, int rb, int cg, int hj) {
  constexpr int NT = (TYPE == T_Q || TYPE == T_KV) ? 8 : 7;
  const int tid = threadIdx.x, lane = tid & 63;
  const int wave = __builtin_amdgcn_readfirstlane(tid >> 6);
  const int wm = wave & 3, wn = wave >> 2;
  const int g = lane >> 4, l15 = lane & 15, q4 = (lane >> 2) & 3, p4 = lane & 3;
  const int row0 = rb * 128;
  const unsigned ldsb = (unsigned)(size_t)lds;

  int baddr[NT];
#pragma unroll
  for (int nt = 0; nt < NT; ++nt) {
    int n0;
    if (TYPE == T_Q) n0 = 28 * (nt >> 1) + 16 * (nt & 1);
    else if (TYPE == T_KV) n0 = (nt < 4 ? 0 : 56) + 28 * ((nt & 3) >> 1) + 16 * (nt & 1);
    else n0 = 16 * nt;
    int byte = 2 * (n0 + 4 * p4);
    int sw = q4 | ((g & 1) << 2);
    baddr[nt] = wn * 16384 + (8 * g + q4) * 256 + (((byte >> 4) ^ (sw << 1)) << 4) + (byte & 15);
  }
  int caddr0 = baddr[0], caddr1 = baddr[1];
#pragma unroll
  for (int w = 1; w < 4; ++w) {
    caddr0 = (wm == w) ? baddr[2 * w] : caddr0;
    caddr1 = (wm == w) ? baddr[(2 * w + 1 < NT) ? 2 * w + 1 : 2 * w] : caddr1;
  }
  const int xrow = wm * 32 + l15;
  const int xa0 = K1_XOFF + xrow * 128 + ((g ^ ((xrow >> 1) & 7)) << 4), xa1 = xa0 ^ 64;
  const int xs0 = xa0 + wn * 2048, xs1 = xa1 + wn * 2048;
  int baddrH[NT], caddr0H = caddr0 + 65536, caddr1H = caddr1 + 65536;
#pragma unroll
  for (int nt = 0; nt < NT; ++nt) { baddrH[nt] = baddr[nt] + 65536; asm volatile("" : "+v"(baddrH[nt])); }
  asm volatile("" : "+v"(caddr0H), "+v"(caddr1H));

  f32x4 acc[2][NT];
#pragma unroll
  for (int mt = 0; mt < 2; ++mt)
#pragma unroll
    for (int nt = 0; nt < NT; ++nt) acc[mt][nt] = f32x4{0.f, 0.f, 0.f, 0.f};
  f32x4 rs_ = {0.f, 0.f, 0.f, 0.f}, rq_ = rs_, cs0 = rs_, cs1 = rs_;
  const u32x4 ONES = {0x3f803f80u, 0x3f803f80u, 0x3f803f80u, 0x3f803f80u};

  struct Frags { u32x4 x0, x1, xs, c0, c1, b[NT]; };
  Frags fr[2];
  auto step = [&](const Frags& f, Frags& n, bool ld, int cn, int ksn, int sc) {
    const int st = cn % 3;
    const int wo = (st == 2 ? 0 : st * K1_WST) + ksn * 8192;
    const int xo = st * K1_XST;
    const int scc = sc < 0 ? 0 : sc;
    const unsigned dx = ldsb + K1_XOFF + (scc % 3) * K1_XST + wave * 1024;
    const unsigned dw = ldsb + (scc % 3) * K1_WST + wave * 1024;
    const char* xb = (const char*)x + scc * 128;
    const char* wb0 = Wh0 + (size_t)scc * 64 * ldw0;
    const char* wb1 = Wh1 + (size_t)scc * 64 * ldw1;
#pragma unroll
    for (int nt = 0; nt < NT; ++nt) {
      if (ld) {
        if (nt == 0) { n.x0 = *(lds_u4*)(lds + (ksn ? xa1 : xa0) + xo); n.x1 = *(lds_u4*)(lds + (ksn ? xa1 : xa0) + xo + 2048); }
        lds_char* bp = lds + (st == 2 ? baddrH[nt] : baddr[nt]) + wo;
        n.b[nt] = tr_read2(bp, bp + 1024);
        if (nt == 3) { lds_char* cp = lds + (st == 2 ? caddr0H : caddr0) + wo; n.c0 = tr_read2(cp, cp + 1024); }
        if (nt == 4) { lds_char* cp = lds + (st == 2 ? caddr1H : caddr1) + wo; n.c1 = tr_read2(cp, cp + 1024); }
        if (nt == 5) n.xs = *(lds_u4*)(lds + (ksn ? xs1 : xs0) + xo);
      }
      const bool trans = (TYPE == T_Q) || (TYPE == T_Z1) || (TYPE == T_KV && nt < 4);
      acc[0][nt] = trans ? mfma16(f.b[nt], f.x0, acc[0][nt]) : mfma16(f.x0, f.b[nt], acc[0][nt]);
      acc[1][nt] = trans ? mfma16(f.b[nt], f.x1, acc[1][nt]) : mfma16(f.x1, f.b[nt], acc[1][nt]);
      if (nt == 1) cs0 = mfma16(ONES, f.c0, cs0);
      if (nt == 2) cs1 = mfma16(ONES, f.c1, cs1);
      if (nt == 3) rs_ = mfma16(ONES, f.xs, rs_);
      if (nt == 4) rq_ = mfma16(f.xs, f.xs, rq_);
      if (sc >= 0 && nt < 6) {
        if (nt < 2) glds16(xb, xoff[nt], dx + nt * 8192);
        else glds16(nt < 4 ? wb0 : wb1, goff[nt - 2], dw + (nt - 2) * 8192);
      }
      __builtin_amdgcn_sched_barrier(0);
    }
  };
  WAIT_VM(12);
  BARRIER();
  {
    Frags& n = fr[0];
    n.x0 = *(lds_u4*)(lds + xa0); n.x1 = *(lds_u4*)(lds + xa0 + 2048);
#pragma unroll
    for (int nt = 0; nt < NT; ++nt) n.b[nt] = tr_read2(lds + baddr[nt], lds + baddr[nt] + 1024);
    n.c0 = tr_read2(lds + caddr0, lds + caddr0 + 1024);
    n.c1 = tr_read2(lds + caddr1, lds + caddr1 + 1024);
    n.xs = *(lds_u4*)(lds + xs0);
  }
#pragma unroll
  for (int c = 0; c < 7; ++c) {
    step(fr[0], fr[1], true, c, 1, -1);
    if (c < 6) {
      if (c < 5) WAIT_VM(6); else WAIT_VM(0);
      WAIT_LGKM0();
      BARRIER();
      if (c == 5 && TYPE == T_KV) {
        unsigned o0 = min(2 * hj * 1568 + wm * 1024 + lane * 16, 25088 - 16);
        glds16(wphi, o0, ldsb + K1_WPHI + wm * 1024);
      }
    }
    step(fr[1], fr[0], c < 6, c + 1, 0, (c + 3 < 7) ? c + 3 : -1);
  }

  lds_f* cstab = (lds_f*)(lds + K1_CS);
  lds_f* rtab = (lds_f*)(lds + K1_RT);
  lds_f* mtab = (lds_f*)(lds + K1_MT);
  {
    float s = rs_[0], c0 = cs0[0], c1 = cs1[0];
    float q = (p4 == 0) ? rq_[0] : (p4 == 1) ? rq_[1] : (p4 == 2) ? rq_[2] : rq_[3];
    q = (g == q4) ? q : 0.f;
    GRP_RED(q, aadd);
    float mean = s * (1.f / 448.f);
    float var = fmaxf(q * (1.f / 448.f) - mean * mean, 0.f);
    float mb = rbf(mean), vb = rbf(var);
    float sd = rbf(__builtin_amdgcn_sqrtf(rbf(vb + 1e-5f)));
    float r = frcp(sd);
    if (g == 0) {
      cstab[(wn * 8 + 2 * wm) * 16 + l15] = c0;
      cstab[(wn * 8 + 2 * wm + 1) * 16 + l15] = c1;
      rtab[wm * 32 + wn * 16 + l15] = r;
      mtab[wm * 32 + wn * 16 + l15] = -mb * r;
    }
  }
  WAIT_VM(0);
  WAIT_LGKM0();
  BARRIER();

  if (TYPE == T_Q || TYPE == T_KV || TYPE == T_Z1) {
    f32x2 Rr[2], Mr[2];
#pragma unroll
    for (int mt = 0; mt < 2; ++mt) {
      float r = rtab[wm * 32 + mt * 16 + l15], m = mtab[wm * 32 + mt * 16 + l15];
      Rr[mt] = f32x2{r, r}; Mr[mt] = f32x2{m, m};
    }
    if (TYPE == T_Z1) {
#pragma unroll
      for (int nt = 0; nt < NT; ++nt) {
        f32x4 c4 = *(lds_f4*)(cstab + (wn * 8 + nt) * 16 + 4 * g);
        const int f = hj * 112 + nt * 16 + 4 * g;
        const int R = (f & 31) >> 3, hh = (f >> 2) & 1;
#pragma unroll
        for (int mt = 0; mt < 2; ++mt) {
          f32x2 a = gelu2(pk_fma(lo2(c4), Mr[mt], lo2(acc[mt][nt]) * Rr[mt]));
          f32x2 b = gelu2(pk_fma(hi2(c4), Mr[mt], hi2(acc[mt][nt]) * Rr[mt]));
          const int s = row0 + wm * 32 + mt * 16 + l15;
          char* dst = ws + WS_Z1B + (size_t)((s >> 5) * 28 + (f >> 5)) * 2048 + R * 512 + ((s & 31) + 32 * hh) * 8;
          st8(dst, u32x2{pack2v(a), pack2v(b)});
        }
      }
      BARRIER();
    } else {
      constexpr int NHW = (TYPE == T_Q) ? 4 : 2;
      const bool inval = (g == 3);
      lds_char* scr = lds + wm * 16384;
      f32x4 pp[2][2][2];
#pragma unroll
      for (int hh = 0; hh < NHW; ++hh) {
        const int head = NHW * hj + hh;
        u32x4 a0, a1;
        if (TYPE == T_KV) {
          lds_char* wp = lds + K1_WPHI + hh * 1568 + (4 * g + q4) * 56 + 8 * p4;
          a0 = tr_read2(wp, wp + 16 * 56);
          a1 = tr_read2(wp + 32, wp + 16 * 56 + 32);
        }
        f32x4 c0 = *(lds_f4*)(cstab + (wn * 8 + 2 * hh) * 16 + 4 * g);
        f32x4 c1 = *(lds_f4*)(cstab + (wn * 8 + 2 * hh + 1) * 16 + 4 * g);
#pragma unroll
        for (int mt = 0; mt < 2; ++mt) {
          u32x4 bq;
          bq[0] = pack2v(pk_fma(lo2(c0), Mr[mt], lo2(acc[mt][2 * hh]) * Rr[mt]));
          bq[1] = pack2v(pk_fma(hi2(c0), Mr[mt], hi2(acc[mt][2 * hh]) * Rr[mt]));
          bq[2] = pack2v(pk_fma(lo2(c1), Mr[mt], lo2(acc[mt][2 * hh + 1]) * Rr[mt]));
          bq[3] = pack2v(pk_fma(hi2(c1), Mr[mt], hi2(acc[mt][2 * hh + 1]) * Rr[mt]));
          bq[2] = inval ? 0u : bq[2]; bq[3] = inval ? 0u : bq[3];
          if (TYPE == T_Q) {
            const int stile = rb * 8 + wm * 2 + mt;
            st16(ws + WS_PHIQ + (size_t)(head * 128 + stile) * 1024 + lane * 16, bq);
          } else {
            f32x4 z4 = {0.f, 0.f, 0.f, 0.f};
            pp[hh & 1][mt][0] = mfma16(a0, bq, z4); pp[hh & 1][mt][1] = mfma16(a1, bq, z4);
          }
        }
      }
      if (TYPE == T_KV) {
        u32x4 vb[2][2];
        f32x4 r4[2], m4[2];
#pragma unroll
        for (int mt = 0; mt < 2; ++mt) {
          r4[mt] = *(lds_f4*)(rtab + wm * 32 + mt * 16 + 4 * g);
          m4[mt] = *(lds_f4*)(mtab + wm * 32 + mt * 16 + 4 * g);
        }
#pragma unroll
        for (int hh = 0; hh < 2; ++hh)
#pragma unroll
          for (int dt = 0; dt < 2; ++dt) {
            const int nt = 4 + 2 * hh + dt;
            float csd = cstab[(wn * 8 + nt) * 16 + l15];
            const f32x2 cc = {csd, csd};
#pragma unroll
            for (int mt = 0; mt < 2; ++mt) {
              vb[hh][dt][2 * mt] = pack2v(pk_fma(cc, lo2(m4[mt]), lo2(acc[mt][nt]) * lo2(r4[mt])));
              vb[hh][dt][2 * mt + 1] = pack2v(pk_fma(cc, hi2(m4[mt]), hi2(acc[mt][nt]) * hi2(r4[mt])));
            }
            if (dt == 1) {
              const unsigned fill = (l15 == 12) ? 0x3f803f80u : 0u;
#pragma unroll
              for (int i = 0; i < 4; ++i) vb[hh][1][i] = (l15 >= 12) ? fill : vb[hh][1][i];
            }
          }
#pragma unroll
        for (int hh = 0; hh < 2; ++hh)
#pragma unroll
          for (int mt = 0; mt < 2; ++mt) {
            u32x4 pos, neg;
            hedgehog_softmax(pp[hh][mt][0], pp[hh][mt][1], inval, pos, neg);
            lds_char* d = scr + hh * 5120 + (mt * 16 + l15) * 160 + 8 * g;
            *(lds_u2*)(d) = u32x2{pos[0], pos[1]};
            *(lds_u2*)(d + 32) = u32x2{pos[2], pos[3]};
            *(lds_u2*)(d + 64) = u32x2{neg[0], neg[1]};
            *(lds_u2*)(d + 96) = u32x2{neg[2], neg[3]};
          }
        f32x4 kv[2][4][2];
#pragma unroll
        for (int hh = 0; hh < 2; ++hh) {
          lds_char* rp = scr + hh * 5120 + (4 * g + q4) * 160 + 8 * p4;
#pragma unroll
          for (int ft = 0; ft < 4; ++ft) {
            u32x4 pa = tr_read2(rp + ft * 32, rp + 16 * 160 + ft * 32);
            f32x4 z4 = {0.f, 0.f, 0.f, 0.f};
            kv[hh][ft][0] = mfma16(pa, vb[hh][0], z4);
            kv[hh][ft][1] = mfma16(pa, vb[hh][1], z4);
          }
        }
        WAIT_LGKM0();
#pragma unroll
        for (int hh = 0; hh < 2; ++hh) {
          lds_char* q = scr + (hh == 0 ? 8192 : 0) + lane * 16;
#pragma unroll
          for (int ft = 0; ft < 4; ++ft) {
            *(lds_f4*)(q + (ft * 2 + 0) * 1024) = kv[hh][ft][0];
            *(lds_f4*)(q + (ft * 2 + 1) * 1024) = kv[hh][ft][1];
          }
        }
      }
      WAIT_LGKM0();
      BARRIER();
      if (TYPE == T_KV) {
#pragma unroll
        for (int it = 0; it < 4; ++it) {
          int pos = it * 256 + tid;
          f32x4 a = *(lds_f4*)(lds + 0 * 16384 + pos * 16);
          a += *(lds_f4*)(lds + 1 * 16384 + pos * 16);
          a += *(lds_f4*)(lds + 2 * 16384 + pos * 16);
          a += *(lds_f4*)(lds + 3 * 16384 + pos * 16);
          int head = 2 * hj + 1 - (pos >> 9);
          st16(ws + WS_KVS + ((size_t)(head * 16 + rb) * 8) * 1024 + (pos & 511) * 16, __builtin_bit_cast(u32x4, a));
        }
      }
    }
  } else {
    lds_f* st = (lds_f*)(lds + K1_ST2);
#pragma unroll
    for (int mt = 0; mt < 2; ++mt) {
      f32x4 r4 = *(lds_f4*)(rtab + wm * 32 + mt * 16 + 4 * g);
      f32x4 m4 = *(lds_f4*)(mtab + wm * 32 + mt * 16 + 4 * g);
      f32x2 sa = {0.f, 0.f}, sb = {0.f, 0.f}, qa = {0.f, 0.f}, qb = {0.f, 0.f};
#pragma unroll
      for (int nt = 0; nt < NT; ++nt) {
        float csd = cstab[(wn * 8 + nt) * 16 + l15];
        const f32x2 cc = {csd, csd};
        f32x2 a = gelu2(pk_fma(cc, lo2(m4), lo2(acc[mt][nt]) * lo2(r4)));
        f32x2 b = gelu2(pk_fma(cc, hi2(m4), hi2(acc[mt][nt]) * hi2(r4)));
        unsigned pa = pack2v(a), pb = pack2v(b);
        sa += a; sb += b; qa = pk_fma(a, a, qa); qb = pk_fma(b, b, qb);
        const int f = hj * 112 + nt * 16 + l15;
        const int s16 = rb * 8 + wm * 2 + mt;
        char* dst = ws + WS_Z2B + (size_t)((f >> 5) * 128 + s16) * 1024 + ((f & 31) + 32 * (g >> 1)) * 16 + (g & 1) * 8;
        st8(dst, u32x2{pa, pb});
      }
      f32x4 o0 = {row_sum16(sa[0]), row_sum16(qa[0]), row_sum16(sa[1]), row_sum16(qa[1])};
      f32x4 o1 = {row_sum16(sb[0]), row_sum16(qb[0]), row_sum16(sb[1]), row_sum16(qb[1])};
      if (l15 == 0) {
        *(lds_f4*)(st + (wn * 128 + wm * 32 + mt * 16 + 4 * g) * 2) = o0;
        *(lds_f4*)(st + (wn * 128 + wm * 32 + mt * 16 + 4 * g) * 2 + 4) = o1;
      }
    }
    WAIT_LGKM0();
    BARRIER();
    if (tid < 128) {
      f32x2 a = *(lds_f2*)(st + tid * 2), b = *(lds_f2*)(st + (128 + tid) * 2);
      *(f32x2*)(ws + WS_STAT + ((size_t)(cg - 10) * 2048 + row0 + tid) * 8) = a + b;
    }
  }
}

__global__ __launch_bounds__(512, 2) void k1_kernel(const __bf16* __restrict__ x, const __bf16* __restrict__ wqkv,
                                                    const __bf16* __restrict__ wphi, const __bf16* __restrict__ win,
                                                    const __bf16* __restrict__ wsp, const __bf16* __restrict__ wo,
                                                    const __bf16* __restrict__ wout, char* __restrict__ ws) {
  extern __shared__ __attribute__((aligned(16))) char smem[];
  lds_char* lds = (lds_char*)smem;
  const int b = blockIdx.x;
  const int xcd = b & 7, idx = b >> 3;
  const int tid = threadIdx.x, lane = tid & 63;
  if (idx >= 28) {
    const int e = xcd * 4 + (idx - 28);
    unsigned acc = 0;
    const u32x4* p = (const u32x4*)wsp + (size_t)e * 16384 + tid;
#pragma unroll 8
    for (int i = 0; i < 32; ++i) { u32x4 v = p[i * 512]; acc ^= v[0] ^ v[3]; }
    const u32x4* q = (const u32x4*)wout + e * 1568 + tid;
#pragma unroll
    for (int i = 0; i < 4; ++i) if (i * 512 + tid < 1568) { u32x4 v = q[i * 512]; acc ^= v[0] ^ v[3]; }
    const u32x4* r = (const u32x4*)wo + e * 784 + tid;
#pragma unroll
    for (int i = 0; i < 2; ++i) if (i * 512 + tid < 784) { u32x4 v = r[i * 512]; acc ^= v[0] ^ v[3]; }
    asm volatile("" :: "v"(acc));
    return;
  }
  const int rb = 2 * xcd + idx / 14, cg = idx % 14;
  const int wave = __builtin_amdgcn_readfirstlane(tid >> 6);
  const unsigned ldsb = (unsigned)(size_t)lds;
  unsigned goff[4], xoff[2];
#pragma unroll
  for (int i = 0; i < 2; ++i) {
    int row = (i * 8 + wave) * 8 + (lane >> 3);
    int c = (lane & 7) ^ ((row >> 1) & 7);
    xoff[i] = (rb * 128 + row) * 896 + c * 16;
  }
  glds16(x, xoff[0], ldsb + K1_XOFF + wave * 1024);
  glds16(x, xoff[1], ldsb + K1_XOFF + wave * 1024 + 8192);
  int ht[2], hj[2];
  k1_half(cg, 0, ht[0], hj[0]);
  k1_half(cg, 1, ht[1], hj[1]);
  const char* Wh[2]; int ldw[2];
#pragma unroll
  for (int h = 0; h < 2; ++h) {
    const bool qkv = ht[h] <= T_KV;
    Wh[h] = uni_ptr(qkv ? (const char*)wqkv : (const char*)win);
    ldw[h] = __builtin_amdgcn_readfirstlane(qkv ? 2688 : 3584);
    const int colA = (ht[h] == T_KV) ? 896 + 112 * hj[h] : 224 * hj[h] + (ht[h] == T_Z2 ? 1792 : 0);
    const int delta = (ht[h] == T_KV) ? 784 : 0, lim = (ht[h] == T_Q) ? 15 : 14;
#pragma unroll
    for (int i = 0; i < 2; ++i) {
      int row = (i * 8 + wave) * 4 + (lane >> 4);
      int sw = (row & 3) | (((row >> 3) & 1) << 2);
      int c = (lane & 15) ^ (sw << 1);
      c = c < lim ? c : 0;
      goff[h * 2 + i] = row * ldw[h] + colA + 16 * c + (c >= 7 ? delta : 0);
    }
  }
#pragma unroll
  for (int chunk = 0; chunk < 3; ++chunk) {
    const unsigned dx = ldsb + K1_XOFF + chunk * K1_XST + wave * 1024;
    const unsigned dw = ldsb + chunk * K1_WST + wave * 1024;
    const char* xb = (const char*)x + chunk * 128;
    if (chunk > 0) {
      glds16(xb, xoff[0], dx);
      glds16(xb, xoff[1], dx + 8192);
    }
#pragma unroll
    for (int i = 0; i < 4; ++i) glds16(Wh[i >> 1] + (size_t)chunk * 64 * ldw[i >> 1], goff[i], dw + i * 8192);
  }
  const int my = (wave >> 2) ? ht[1] : ht[0], mj = (wave >> 2) ? hj[1] : hj[0];
  if (my == T_Q) k1_wave<T_Q>(x, Wh[0], Wh[1], ldw[0], ldw[1], goff, xoff, wphi, ws, lds, rb, cg, mj);
  else if (my == T_KV) k1_wave<T_KV>(x, Wh[0], Wh[1], ldw[0], ldw[1], goff, xoff, wphi, ws, lds, rb, cg, mj);
  else if (my == T_Z1) k1_wave<T_Z1>(x, Wh[0], Wh[1], ldw[0], ldw[1], goff, xoff, wphi, ws, lds, rb, cg, mj);
  else k1_wave<T_Z2>(x, Wh[0], Wh[1], ldw[0], ldw[1], goff, xoff, wphi, ws, lds, rb, cg, mj);
}

constexpr int K2_WB = 0;
constexpr int K2_RT = 32768;
constexpr int K2_MT = K2_RT + 8192;
constexpr int K2_CT = K2_MT + 4096;
constexpr int K2_LDS = K2_CT + 512;

__global__ __launch_bounds__(768, 3) void k2_kernel(const __bf16* __restrict__ wsp, const __bf16* __restrict__ bsp,
                                                    const __bf16* __restrict__ wphi, char* __restrict__ ws) {
  extern __shared__ __attribute__((aligned(16))) char smem[];
  lds_char* lds = (lds_char*)smem;
  const int tid = threadIdx.x, lane = tid & 63;
  const int wave = __builtin_amdgcn_readfirstlane(tid >> 6);
  const int b = blockIdx.x, xcd = b & 7, idx = b >> 3;
  if (idx >= 28) {
    if (wave >= 8) return;
    const int e = xcd * 4 + (idx - 28), head = e >> 1, half = e & 1;
    const int g = lane >> 4, l15 = lane & 15, q4 = (lane >> 2) & 3, p4 = lane & 3;
    u32x4 qf[8];
    const char* pbase = ws + WS_PHIQ + (size_t)(head * 128 + half * 64 + wave * 8) * 1024 + lane * 16;
#pragma unroll
    for (int i = 0; i < 8; ++i) qf[i] = *(const u32x4*)(pbase + i * 1024);
    if (tid < 128) {
      const unsigned o = min(head * 1568 + tid * 16, 25088 - 16);
      *(lds_u4*)(lds + 8192 + tid * 16) = *(const u32x4*)((const char*)wphi + o);
    }
    {
      const char* kb = ws + WS_KVS + ((size_t)(head * 16) * 8 + wave) * 1024 + lane * 16;
      f32x4 a = {0.f, 0.f, 0.f, 0.f};
#pragma unroll
      for (int r = 0; r < 16; ++r) a += *(const f32x4*)(kb + (size_t)r * 8192);
      *(lds_u2*)(lds + wave * 512 + lane * 8) = u32x2{pack2(a[0], a[1]), pack2(a[2], a[3])};
    }
    __syncthreads();
    u32x4 af[2][2];
#pragma unroll
    for (int sg = 0; sg < 2; ++sg)
#pragma unroll
      for (int dt = 0; dt < 2; ++dt) {
        u32x2 lo = *(lds_u2*)(lds + ((2 * sg) * 2 + dt) * 512 + lane * 8);
        u32x2 hi = *(lds_u2*)(lds + ((2 * sg + 1) * 2 + dt) * 512 + lane * 8);
        af[sg][dt] = u32x4{lo[0], lo[1], hi[0], hi[1]};
      }
    lds_char* wp = lds + 8192 + (4 * g + q4) * 56 + 8 * p4;
    const u32x4 a0 = tr_read2(wp, wp + 16 * 56), a1 = tr_read2(wp + 32, wp + 16 * 56 + 32);
    const bool inval = (g == 3);
#pragma unroll
    for (int i = 0; i < 8; ++i) {
      f32x4 z4 = {0.f, 0.f, 0.f, 0.f};
      f32x4 p0 = mfma16(a0, qf[i], z4), p1 = mfma16(a1, qf[i], z4);
      u32x4 pos, neg;
      hedgehog_softmax(p0, p1, inval, pos, neg);
      f32x4 n0 = mfma16(af[0][0], pos, z4); n0 = mfma16(af[1][0], neg, n0);
      f32x4 n1 = mfma16(af[0][1], pos, z4); n1 = mfma16(af[1][1], neg, n1);
      float den = __shfl(n1[0], 48 + l15);
      den = rbf(rbf(den) + 1e-6f);
      float rd = 1.f / den;
      const int s = half * 1024 + (wave * 8 + i) * 16 + l15;
#pragma unroll
      for (int dt = 0; dt < 2; ++dt) {
        const f32x4& n = dt ? n1 : n0;
        u32x2 o = {pack2(rbf(n[0]) * rd, rbf(n[1]) * rd), pack2(rbf(n[2]) * rd, rbf(n[3]) * rd)};
        const int k = 28 * head + 16 * dt + 4 * g;
        const int kl = k & 15, hh = (kl >> 2) & 1, j0 = (kl >> 3) * 4;
        char* dst = ws + WS_HO + (size_t)((s >> 5) * 84 + (k >> 4)) * 1024 + ((s & 31) + 32 * hh) * 16 + j0 * 2;
        if (!(dt == 1 && g == 3)) st8(dst, o);
      }
    }
    return;
  }

  const int rb = 4 * xcd + idx / 7, ct = idx % 7;
  constexpr int NK = 16;
  const int koff = (ct * 2 + (ct > 3 ? 1 : 0)) & 15;
#define KROT(k) ((((k) + koff) & 15))
  auto stat_load = [&](int row, f32x2 (&a)[4]) {
#pragma unroll
    for (int gq = 0; gq < 4; ++gq) a[gq] = *(const f32x2*)(ws + WS_STAT + ((size_t)gq * 2048 + row) * 8);
  };
  auto stat_put = [&](int row, const f32x2 (&a)[4]) {
    f32x2 t = (a[0] + a[1]) + (a[2] + a[3]);
    float mean = t[0] * (1.f / 896.f);
    float var = fmaxf(t[1] * (1.f / 896.f) - mean * mean, 0.f);
    float vb = rbf(var);
    float sd = rbf(__builtin_amdgcn_sqrtf(rbf(vb + 1e-5f)));
    *(lds_f*)(lds + K2_RT + row * 4) = frcp(sd);
    *(__attribute__((address_space(3))) unsigned short*)(lds + K2_MT + row * 2) = (unsigned short)pack2(mean, 0.f);
  };
  const int srow = KROT(tid >> 7) * 128 + (tid & 127);
  f32x2 sa0[4];
  if (wave >= 4) {
    __builtin_amdgcn_s_setprio(2);
    const int pt = tid - 256, t = pt >> 4, cp = pt & 15;
    const int g = lane >> 4, l15 = lane & 15;
    const char* src = (const char*)wsp + (size_t)(rb * 64 + t) * 4096 + cp * 16;
    constexpr int PD = 4;
    u32x4 wr[PD][2];
#define WLOAD(dst, kk) do { _Pragma("unroll") for (int j = 0; j < 2; ++j) \
      dst[j] = *(const u32x4*)(src + KROT(kk) * 256 + j * (32 * 4096)); } while (0)
    WLOAD(wr[0], 0);
    if (tid < 512) {
      stat_load(srow, sa0);
      __builtin_amdgcn_sched_barrier(0);
      stat_put(srow, sa0);
    }
    const int wa = t * 256 + ((cp ^ (t & 15)) << 4);
    const int ta = cp * 32;
    const int pw = wave - 4;
    const int crow = (pw >> 1) * 16 + l15;
    const int ca = (crow * 256 + ((g ^ l15) << 4)) ^ ((pw & 1) << 7);
    f32x4 cacc = {0.f, 0.f, 0.f, 0.f};
    WAIT_LGKM0(); BARRIER();
    f32x4 rr[2][2];
    rr[0][0] = *(lds_f4*)(lds + K2_RT + KROT(0) * 512 + ta); rr[0][1] = *(lds_f4*)(lds + K2_RT + KROT(0) * 512 + ta + 16);
    WLOAD(wr[1], 1);
    f32x2 sb[3][4];
    __builtin_amdgcn_sched_barrier(0);
#pragma unroll
    for (int k = 0; k <= NK; ++k) {
      if (k == 1) {
#pragma unroll
        for (int i = 0; i < 3; ++i) stat_load(KROT(4 + 4 * i + (pt >> 7)) * 128 + (pt & 127), sb[i]);
      }
      lds_char* wb = lds + K2_WB + (k & 1) * 16384;
      lds_char* pb = lds + K2_WB + ((k + 1) & 1) * 16384;
      u32x4 mf[2], wf[2];
#pragma unroll
      for (int j = 0; j < 2; ++j) {
        if (k > 0) {
          mf[j] = *(lds_u4*)(lds + K2_MT + (KROT(k - 1) * 128 + ((pw & 1) * 2 + j) * 32 + g * 8) * 2);
          wf[j] = *(lds_u4*)(pb + (ca ^ (j << 6)));
        }
      }
      if (k + 1 < NK) {
        rr[(k + 1) & 1][0] = *(lds_f4*)(lds + K2_RT + KROT(k + 1) * 512 + ta);
        rr[(k + 1) & 1][1] = *(lds_f4*)(lds + K2_RT + KROT(k + 1) * 512 + ta + 16);
      }
      __builtin_amdgcn_sched_barrier(0);
#pragma unroll
      for (int j = 0; j < 2; ++j) {
        if (k < NK) {
          const f32x4 r0 = rr[k & 1][0], r1 = rr[k & 1][1];
          u32x4 w = wr[k % PD][j];
          if (k + PD < NK) wr[k % PD][j] = *(const u32x4*)(src + KROT(k + PD) * 256 + j * (32 * 4096));
          u32x4 o;
          o[0] = pack2(smul(bf_lo(w[0]), r0[0]), smul(bf_hi(w[0]), r0[1]));
          o[1] = pack2(smul(bf_lo(w[1]), r0[2]), smul(bf_hi(w[1]), r0[3]));
          o[2] = pack2(smul(bf_lo(w[2]), r1[0]), smul(bf_hi(w[2]), r1[1]));
          o[3] = pack2(smul(bf_lo(w[3]), r1[2]), smul(bf_hi(w[3]), r1[3]));
          *(lds_u4*)(wb + wa + j * 8192) = o;
        }
        __builtin_amdgcn_sched_barrier(0);
        if (k > 0) cacc = mfma16(mf[j], wf[j], cacc);
      }
      if (k == 0) { WLOAD(wr[2], 2); WLOAD(wr[3], 3); }
      if (k == 2) {
#pragma unroll
        for (int i = 0; i < 3; ++i) stat_put(KROT(4 + 4 * i + (pt >> 7)) * 128 + (pt & 127), sb[i]);
      }
      if (k == NK) {
        if (g == 0) *(lds_f*)(lds + K2_CT + ((pw & 1) * 64 + crow) * 4) = cacc[0];
      }
      WAIT_LGKM0();
      BARRIER();
    }
  } else {
    const int F = 4 * ct + wave, l31 = lane & 31, hh = lane >> 5;
    const char* zsrc = ws + WS_Z2B + (size_t)F * 128 * 1024 + lane * 16;
    constexpr int ZD = 16;
    u32x4 zf[ZD];
#pragma unroll
    for (int i = 0; i < 8; ++i) zf[i] = *(const u32x4*)(zsrc + (KROT(i >> 3) * 8 + (i & 7)) * 1024);
    stat_load(srow, sa0);
    __builtin_amdgcn_sched_barrier(0);
    stat_put(srow, sa0);
    WAIT_LGKM0(); BARRIER();
    const int ra = l31 * 256 + ((hh ^ (l31 & 15)) << 4);
    f32x16 acc[2];
#pragma unroll
    for (int i = 0; i < 16; ++i) { acc[0][i] = 0.f; acc[1][i] = 0.f; }
    u32x2 z1v[2][4];
    float bb[2];
    BARRIER();
    int rk[8];
#pragma unroll
    for (int kk = 0; kk < 8; ++kk) { rk[kk] = ra ^ (kk << 5); }
    constexpr int LD = 3;
    u32x4 wq[LD + 1][2];
#pragma unroll
    for (int i = 0; i < LD; ++i) { wq[i][0] = *(lds_u4*)(lds + rk[i]); wq[i][1] = *(lds_u4*)(lds + rk[i] + 8192); }
#pragma unroll
    for (int k = 0; k < NK; ++k) {
#pragma unroll
      for (int kk = 0; kk < 8; ++kk) {
        const int ks = k * 8 + kk;
        if (kk == 8 - LD && k + 1 < NK) { WAIT_LGKM0(); BARRIER(); }
        if (ks + LD < 128) {
          const int nb = (((ks + LD) >> 3) & 1) * 16384;
          wq[(ks + LD) % (LD + 1)][0] = *(lds_u4*)(lds + rk[(kk + LD) & 7] + nb);
          wq[(ks + LD) % (LD + 1)][1] = *(lds_u4*)(lds + rk[(kk + LD) & 7] + nb + 8192);
        }
        const u32x4 w0 = wq[ks % (LD + 1)][0], w1 = wq[ks % (LD + 1)][1];
        if (ks == 108) {
#pragma unroll
          for (int tt = 0; tt < 2; ++tt) {
            const int t = rb * 64 + tt * 32 + l31;
            bb[tt] = bf_lo((unsigned)*(const unsigned short*)(bsp + t));
            const char* z1p = ws + WS_Z1B + (size_t)((t >> 5) * 28 + F) * 2048 + lane * 8;
#pragma unroll
            for (int R = 0; R < 4; ++R) z1v[tt][R] = *(const u32x2*)(z1p + R * 512);
          }
        }
        u32x4 z = zf[ks % ZD];
#define ZLOAD(i) zf[(i) % ZD] = *(const u32x4*)(zsrc + (KROT((i) >> 3) * 8 + ((i) & 7)) * 1024)
        if (ks < ZD - 8) { ZLOAD(8 + 2 * ks); ZLOAD(9 + 2 * ks); }
        else if (ks + ZD < 128) ZLOAD(ks + ZD);
        acc[0] = mfma32(z, w0, acc[0]);
        acc[1] = mfma32(z, w1, acc[1]);
        __builtin_amdgcn_sched_barrier(0);
      }
    }
    WAIT_LGKM0();
    BARRIER();
#pragma unroll
    for (int tt = 0; tt < 2; ++tt) {
      const int t = rb * 64 + tt * 32 + l31;
      const float c = *(lds_f*)(lds + K2_CT + (tt * 32 + l31) * 4) + *(lds_f*)(lds + K2_CT + (64 + tt * 32 + l31) * 4);
      const f32x2 cc = {c, c}, b2 = {bb[tt], bb[tt]};
      unsigned hp[8];
#pragma unroll
      for (int R = 0; R < 4; ++R) {
        f32x2 g0 = unpack2(pack2v(f32x2{acc[tt][4 * R + 0], acc[tt][4 * R + 1]} - cc));
        f32x2 g1 = unpack2(pack2v(f32x2{acc[tt][4 * R + 2], acc[tt][4 * R + 3]} - cc));
        g0 = unpack2(pack2v(g0 + b2)); g1 = unpack2(pack2v(g1 + b2));
        hp[2 * R] = pack2v(unpack2(z1v[tt][R][0]) * g0); hp[2 * R + 1] = pack2v(unpack2(z1v[tt][R][1]) * g1);
      }
      char* dst = ws + WS_HO + (size_t)((t >> 5) * 84 + 28 + 2 * F) * 1024 + lane * 16;
      st16(dst, u32x4{hp[0], hp[1], hp[2], hp[3]});
      st16(dst + 1024, u32x4{hp[4], hp[5], hp[6], hp[7]});
    }
  }
}

constexpr int K3_STG = 16384;
constexpr int K3_NB = 5;
constexpr int K3_LDS = K3_STG * K3_NB;

__global__ __launch_bounds__(256, 2) void k3_kernel(const __bf16* __restrict__ x, const __bf16* __restrict__ wo,
                                                    const __bf16* __restrict__ wout, char* __restrict__ ws,
                                                    __bf16* __restrict__ out) {
  extern __shared__ __attribute__((aligned(16))) char smem[];
  lds_char* lds = (lds_char*)smem;
  const unsigned ldsb = (unsigned)(size_t)lds;
  const int tid = threadIdx.x, lane = tid & 63;
  const int wave = __builtin_amdgcn_readfirstlane(tid >> 6);
  const int b = blockIdx.x, xcd = b & 7, idx = b >> 3;
  const int rb = 4 * xcd + idx / 7, ct = idx % 7;
  const int wt = wave & 1, wn = wave >> 1;
  const int l31 = lane & 31, hh = lane >> 5, gi = lane >> 4, q4 = (lane >> 2) & 3, p4 = lane & 3;

  unsigned goff[2];
#pragma unroll
  for (int i = 0; i < 2; ++i) {
    int row = (i * 4 + wave) * 8 + (lane >> 3);
    int sw = ((row & 3) << 1) ^ (((row >> 3) & 1));
    int c = (lane & 7) ^ sw;
    goff[i] = row * 896 + ct * 128 + c * 16;
  }
  const char* abase = ws + WS_HO + (size_t)(rb * 2) * 84 * 1024;
  const unsigned aoff = wave * 1024 + lane * 16;
  auto stage = [&](int chunk) {
    const char* base = (chunk < 7) ? ((const char*)wo + (size_t)chunk * 64 * 896) : ((const char*)wout + (size_t)(chunk - 7) * 64 * 896);
    const unsigned dst = ldsb + (chunk % K3_NB) * K3_STG + wave * 1024;
    glds16(abase + chunk * 4096, aoff, dst);
    glds16(abase + chunk * 4096 + 84 * 1024, aoff, dst + 4096);
    glds16(base, goff[0], dst + 8192);
    glds16(base, goff[1], dst + 8192 + 4096);
  };
  const int t = rb * 64 + wt * 32 + l31;
  u32x2 xv[4];
  {
    const unsigned xo = (unsigned)(t * D_ + ct * 64 + wn * 32 + 4 * hh) * 2;
    asm volatile("global_load_dwordx2 %0, %4, %5\n\tglobal_load_dwordx2 %1, %4, %5 offset:16\n\t"
                 "global_load_dwordx2 %2, %4, %5 offset:32\n\tglobal_load_dwordx2 %3, %4, %5 offset:48"
                 : "=&v"(xv[0]), "=&v"(xv[1]), "=&v"(xv[2]), "=&v"(xv[3]) : "v"(xo), "s"(x) : "memory");
  }
  stage(0); stage(1); stage(2); stage(3);

  int baddr;
  {
    int row = 4 * (gi >> 1) + q4;
    int byte = 2 * (wn * 32 + (gi & 1) * 16 + 4 * p4);
    int sw = ((row & 3) << 1);
    baddr = 8192 + row * 128 + (((byte >> 4) ^ sw) << 4) + (byte & 15);
  }
  const int baddr2 = (baddr ^ 16) + 8 * 128;
  const int aaddr = wt * 4096 + lane * 16;

  f32x16 acc;
#pragma unroll
  for (int i = 0; i < 16; ++i) acc[i] = 0.f;
  unsigned t1[8];
  asm volatile("s_waitcnt vmcnt(12)" : "+v"(xv[0]), "+v"(xv[1]), "+v"(xv[2]), "+v"(xv[3]) :: "memory");
  BARRIER();
  u32x4 wf = tr_read2(lds + baddr, lds + baddr2), af = *(lds_u4*)(lds + aaddr);
#pragma unroll
  for (int c = 0; c < 21; ++c) {
#pragma unroll
    for (int kk = 0; kk < 4; ++kk) {
      u32x4 wn_ = wf, an_ = af;
      if (kk == 3 && c + 1 < 21) {
        if (c + 3 < 21) WAIT_VM(8); else if (c + 2 < 21) WAIT_VM(4); else WAIT_VM(0);
        WAIT_LGKM0();
        BARRIER();
        if (c + 4 < 21) stage(c + 4);
      }
      if (c * 4 + kk + 1 < 84) {
        const int cn = (kk == 3) ? c + 1 : c, kn = (kk + 1) & 3;
        lds_char* bufp = lds + (cn % K3_NB) * K3_STG;
        wn_ = tr_read2(bufp + baddr + kn * 2048, bufp + baddr2 + kn * 2048);
        an_ = *(lds_u4*)(bufp + aaddr + kn * 1024);
      }
      acc = mfma32(wf, af, acc);
      wf = wn_; af = an_;
      __builtin_amdgcn_sched_barrier(0);
    }
    if (c == 6) {
#pragma unroll
      for (int R = 0; R < 4; ++R) {
        f32x2 a0 = unpack2(pack2v(f32x2{acc[4 * R + 0], acc[4 * R + 1]}));
        f32x2 a1 = unpack2(pack2v(f32x2{acc[4 * R + 2], acc[4 * R + 3]}));
        t1[2 * R] = pack2v(unpack2(xv[R][0]) + a0); t1[2 * R + 1] = pack2v(unpack2(xv[R][1]) + a1);
      }
#pragma unroll
      for (int i = 0; i < 16; ++i) acc[i] = 0.f;
    }
  }
  __bf16* orow = out + (size_t)t * D_ + ct * 64 + wn * 32 + 4 * hh;
#pragma unroll
  for (int R = 0; R < 4; ++R) {
    f32x2 a0 = unpack2(pack2v(f32x2{acc[4 * R + 0], acc[4 * R + 1]}));
    f32x2 a1 = unpack2(pack2v(f32x2{acc[4 * R + 2], acc[4 * R + 3]}));
    *(u32x2*)(orow + 8 * R) = u32x2{pack2v(unpack2(t1[2 * R]) + a0), pack2v(unpack2(t1[2 * R + 1]) + a1)};
  }
}

extern "C" void kernel_launch(void* const* d_in, const int* in_sizes, int n_in, void* d_out, int out_size,
                              void* d_ws, size_t ws_size, hipStream_t stream) {
  const __bf16* x = (const __bf16*)d_in[0];
  const __bf16* wqkv = (const __bf16*)d_in[1];
  const __bf16* wphi = (const __bf16*)d_in[2];
  const __bf16* wo = (const __bf16*)d_in[3];
  const __bf16* win = (const __bf16*)d_in[5];
  const __bf16* wsp = (const __bf16*)d_in[6];
  const __bf16* bsp = (const __bf16*)d_in[7];
  const __bf16* wout = (const __bf16*)d_in[8];
  char* ws = (char*)d_ws;
  (void)hipFuncSetAttribute((const void*)k1_kernel, hipFuncAttributeMaxDynamicSharedMemorySize, K1_LDS);
  (void)hipFuncSetAttribute((const void*)k3_kernel, hipFuncAttributeMaxDynamicSharedMemorySize, K3_LDS);
  k1_kernel<<<256, 512, K1_LDS, stream>>>(x, wqkv, wphi, win, wsp, wo, wout, ws);
  k2_kernel<<<256, 768, K2_LDS, stream>>>(wsp, bsp, wphi, ws);
  k3_kernel<<<224, 256, K3_LDS, stream>>>(x, wo, wout, ws, (__bf16*)d_out);
}
```

```cpp
#include <hip/hip_runtime.h>
#include <hip/hip_bf16.h>
#include <stdint.h>

#define DI __device__ __forceinline__
typedef __attribute__((ext_vector_type(8))) __bf16 bf16x8;
typedef __attribute__((ext_vector_type(2))) __bf16 bf16x2;
typedef __attribute__((ext_vector_type(4))) short s16x4;
typedef __attribute__((ext_vector_type(4))) float f32x4;
typedef __attribute__((ext_vector_type(2))) float f32x2;
typedef __attribute__((ext_vector_type(16))) float f32x16;
typedef __attribute__((ext_vector_type(4))) unsigned u32x4;
typedef __attribute__((ext_vector_type(2))) unsigned u32x2;
typedef __attribute__((address_space(3))) char lds_char;
typedef __attribute__((address_space(3))) s16x4 lds_s16x4;

extern __shared__ __attribute__((aligned(16))) char smem[];

constexpr int S_ = 2048, D_ = 448;

constexpr size_t WS_PHIQ = 0;
constexpr size_t WS_KVS = 4u << 20;
constexpr size_t WS_Z1B = 6u << 20;
constexpr size_t WS_Z2B = 10u << 20;
constexpr size_t WS_STAT = 14u << 20;
constexpr size_t WS_HO = 15u << 20;

DI unsigned pack2(float a, float b) {
  f32x2 v = {a, b};
  return __builtin_bit_cast(unsigned, __builtin_convertvector(v, bf16x2));
}
DI float bf_lo(unsigned u) { return __uint_as_float(u << 16); }
DI float bf_hi(unsigned u) { return __uint_as_float(u & 0xffff0000u); }
DI float rbf(float x) { return bf_lo(pack2(x, 0.f)); }
DI f32x4 mfma16(u32x4 a, u32x4 b, f32x4 c) {
  return __builtin_amdgcn_mfma_f32_16x16x32_bf16(__builtin_bit_cast(bf16x8, a), __builtin_bit_cast(bf16x8, b), c, 0, 0, 0);
}
DI f32x16 mfma32(u32x4 a, u32x4 b, f32x16 c) {
  return __builtin_amdgcn_mfma_f32_32x32x16_bf16(__builtin_bit_cast(bf16x8, a), __builtin_bit_cast(bf16x8, b), c, 0, 0, 0);
}
DI u32x2 tr_read(lds_char* p) {
  s16x4 r = __builtin_amdgcn_ds_read_tr16_b64_v4i16((lds_s16x4*)p);
  return __builtin_bit_cast(u32x2, r);
}
DI u32x4 tr_read2(lds_char* p0, lds_char* p1) {
  u32x2 a = tr_read(p0), b = tr_read(p1);
  u32x4 r = {a[0], a[1], b[0], b[1]};
  return r;
}
DI float frcp(float x) { return __builtin_amdgcn_rcpf(x); }
DI const char* uni_ptr(const char* p) {
  unsigned long long v = (unsigned long long)p;
  unsigned lo = __builtin_amdgcn_readfirstlane((unsigned)v), hi = __builtin_amdgcn_readfirstlane((unsigned)(v >> 32));
  return (const char*)(((unsigned long long)hi << 32) | lo);
}
DI void glds16(const void* sbase, unsigned voff, unsigned lds_dst) {
  asm volatile("s_mov_b32 m0, %2\n\ts_nop 3\n\tglobal_load_lds_dwordx4 %0, %1"
               :: "v"(voff), "s"(sbase), "s"(lds_dst) : "memory");
}
DI void st16(void* p, u32x4 v) {
  asm volatile("global_store_dwordx4 %0, %1, off sc0 sc1\n\ts_nop 1" :: "v"(p), "v"(v) : "memory");
}
DI void st8(void* p, u32x2 v) {
  asm volatile("global_store_dwordx2 %0, %1, off sc0 sc1\n\ts_nop 1" :: "v"(p), "v"(v) : "memory");
}
#define WAIT_VMI(n) asm volatile("s_waitcnt vmcnt(%0)" :: "i"(n) : "memory")
#define WAIT_VM(n) asm volatile("s_waitcnt vmcnt(" #n ")" ::: "memory")
#define WAIT_LGKM0() asm volatile("s_waitcnt lgkmcnt(0)" ::: "memory")
#define BARRIER() __builtin_amdgcn_s_barrier()

constexpr int K1_WST = 32768;
constexpr int K1_XOFF = 98304;
constexpr int K1_XST = 16384;
constexpr int K1_WPHI = 131072;
constexpr int K1_CS = K1_WPHI + 13312;
constexpr int K1_RT = K1_CS + 1024;
constexpr int K1_MT = K1_RT + 512;
constexpr int K1_ST2 = K1_MT + 512;
constexpr int K1_LDS = 147456 + 2048;

enum { T_Q = 0, T_KV = 1, T_Z1 = 2, T_Z2 = 3 };
constexpr int K2_CODE_BYTES = 28672, K3_CODE_BYTES = 6656;

typedef __attribute__((address_space(3))) float lds_f;
typedef __attribute__((address_space(3))) f32x4 lds_f4;
typedef __attribute__((address_space(3))) f32x2 lds_f2;
typedef __attribute__((address_space(3))) u32x4 lds_u4;
typedef __attribute__((address_space(3))) u32x2 lds_u2;

DI float amax(float a, float b) { float r; asm("v_max_f32 %0, %1, %2" : "=v"(r) : "v"(a), "v"(b)); return r; }
DI float amin(float a, float b) { float r; asm("v_min_f32 %0, %1, %2" : "=v"(r) : "v"(a), "v"(b)); return r; }
#define GRP_RED(x, OP) do { \
    auto r_ = __builtin_amdgcn_permlane16_swap(__float_as_uint(x), __float_as_uint(x), false, false); \
    x = OP(__uint_as_float(r_[0]), __uint_as_float(r_[1])); \
    auto q_ = __builtin_amdgcn_permlane32_swap(__float_as_uint(x), __float_as_uint(x), false, false); \
    x = OP(__uint_as_float(q_[0]), __uint_as_float(q_[1])); } while (0)
DI float aadd(float a, float b) { return a + b; }
DI float smul(float a, float b) { float r; asm("v_mul_f32 %0, %1, %2" : "=v"(r) : "v"(a), "v"(b)); return r; }
DI f32x2 pk_fma(f32x2 a, f32x2 b, f32x2 c) { return __builtin_elementwise_fma(a, b, c); }
DI f32x2 lo2(const f32x4& v) { return f32x2{v[0], v[1]}; }
DI f32x2 hi2(const f32x4& v) { return f32x2{v[2], v[3]}; }
DI unsigned pack2v(f32x2 v) { return __builtin_bit_cast(unsigned, __builtin_convertvector(v, bf16x2)); }
DI f32x2 unpack2(unsigned u) { return f32x2{bf_lo(u), bf_hi(u)}; }
DI f32x2 gelu2(f32x2 v) {
  const f32x2 c1 = {-2.302208198f, -2.302208198f};
  const f32x2 c2 = {-0.1029432397f, -0.1029432397f};
  f32x2 u = v * pk_fma(v * v, c2, c1);
  f32x2 d = f32x2{__builtin_amdgcn_exp2f(u[0]), __builtin_amdgcn_exp2f(u[1])} + f32x2{1.f, 1.f};
  return v * f32x2{frcp(d[0]), frcp(d[1])};
}
DI float row_sum16(float x) {
  x += __builtin_bit_cast(float, __builtin_amdgcn_update_dpp(0, __builtin_bit_cast(int, x), 0x128, 0xf, 0xf, false));
  x += __builtin_bit_cast(float, __builtin_amdgcn_update_dpp(0, __builtin_bit_cast(int, x), 0x124, 0xf, 0xf, false));
  x += __builtin_bit_cast(float, __builtin_amdgcn_update_dpp(0, __builtin_bit_cast(int, x), 0x122, 0xf, 0xf, false));
  x += __builtin_bit_cast(float, __builtin_amdgcn_update_dpp(0, __builtin_bit_cast(int, x), 0x121, 0xf, 0xf, false));
  return x;
}

DI void hedgehog_softmax(const f32x4& p0, const f32x4& p1, bool inval, u32x4& pos, u32x4& neg) {
  f32x2 v[4];
  v[0] = unpack2(pack2v(lo2(p0))); v[1] = unpack2(pack2v(hi2(p0)));
  v[2] = unpack2(pack2v(lo2(p1))); v[3] = unpack2(pack2v(hi2(p1)));
  f32x2 w2 = inval ? v[0] : v[2], w3 = inval ? v[1] : v[3];
  float mx = __builtin_fmaxf(__builtin_fmaxf(__builtin_fmaxf(v[0][0], v[0][1]), __builtin_fmaxf(v[1][0], v[1][1])),
                             __builtin_fmaxf(__builtin_fmaxf(w2[0], w2[1]), __builtin_fmaxf(w3[0], w3[1])));
  float mn = __builtin_fminf(__builtin_fminf(__builtin_fminf(v[0][0], v[0][1]), __builtin_fminf(v[1][0], v[1][1])),
                             __builtin_fminf(__builtin_fminf(w2[0], w2[1]), __builtin_fminf(w3[0], w3[1])));
  GRP_RED(mx, amax);
  GRP_RED(mn, amin);
  const float L = 1.4426950408889634f;
  const f32x2 L2 = {L, L}, nL2 = {-L, -L};
  const f32x2 bx = {-mx * L, -mx * L}, bn = {mn * L, mn * L};
  f32x2 e1[4], e2[4];
#pragma unroll
  for (int i = 0; i < 4; ++i) {
    f32x2 a = pk_fma(v[i], L2, bx), b = pk_fma(v[i], nL2, bn);
    e1[i] = f32x2{__builtin_amdgcn_exp2f(a[0]), __builtin_amdgcn_exp2f(a[1])};
    e2[i] = f32x2{__builtin_amdgcn_exp2f(b[0]), __builtin_amdgcn_exp2f(b[1])};
  }
  const f32x2 zz = {0.f, 0.f};
  e1[2] = inval ? zz : e1[2]; e1[3] = inval ? zz : e1[3];
  e2[2] = inval ? zz : e2[2]; e2[3] = inval ? zz : e2[3];
  f32x2 t1 = (e1[0] + e1[1]) + (e1[2] + e1[3]), t2 = (e2[0] + e2[1]) + (e2[2] + e2[3]);
  float s1 = t1[0] + t1[1], s2 = t2[0] + t2[1];
  GRP_RED(s1, aadd);
  GRP_RED(s2, aadd);
  float r1 = frcp(s1), r2 = frcp(s2);
  const f32x2 R1 = {r1, r1}, R2 = {r2, r2};
#pragma unroll
  for (int i = 0; i < 4; ++i) { pos[i] = pack2v(e1[i] * R1); neg[i] = pack2v(e2[i] * R2); }
}

DI void k1_half(int cg, int wn, int& ht, int& j) {
  const int lo = (wn == 0) ? T_KV : (cg < 4 ? T_Q : T_Z1);
  ht = cg < 8 ? lo : (cg < 10 ? T_Z1 : T_Z2);
  const int jlo = (wn == 0 || cg < 4) ? cg : cg - 4;
  j = cg < 8 ? jlo : (cg < 10 ? 2 * cg - 12 + wn : 2 * cg - 20 + wn);
}

template <int TYPE>
DI void k1_wave(const __bf16* __restrict__ x, const char* Wh0, const char* Wh1, int ldw0, int ldw1,
                const unsigned (&goff)[4], const unsigned (&xoff)[2], const __bf16* __restrict__ wphi,
                char* __restrict__ ws, lds_char* lds, int rb, int cg, int hj) {
  constexpr int NT = (TYPE == T_Q || TYPE == T_KV) ? 8 : 7;
  const int tid = threadIdx.x, lane = tid & 63;
  const int wave = __builtin_amdgcn_readfirstlane(tid >> 6);
  const int wm = wave & 3, wn = wave >> 2;
  const int g = lane >> 4, l15 = lane & 15, q4 = (lane >> 2) & 3, p4 = lane & 3;
  const int row0 = rb * 128;
  const unsigned ldsb = (unsigned)(size_t)lds;

  int baddr[NT];
#pragma unroll
  for (int nt = 0; nt < NT; ++nt) {
    int n0;
    if (TYPE == T_Q) n0 = 28 * (nt >> 1) + 16 * (nt & 1);
    else if (TYPE == T_KV) n0 = (nt < 4 ? 0 : 56) + 28 * ((nt & 3) >> 1) + 16 * (nt & 1);
    else n0 = 16 * nt;
    int byte = 2 * (n0 + 4 * p4);
    int sw = q4 | ((g & 1) << 2);
    baddr[nt] = wn * 16384 + (8 * g + q4) * 256 + (((byte >> 4) ^ (sw << 1)) << 4) + (byte & 15);
  }
  int caddr0 = baddr[0], caddr1 = baddr[1];
#pragma unroll
  for (int w = 1; w < 4; ++w) {
    caddr0 = (wm == w) ? baddr[2 * w] : caddr0;
    caddr1 = (wm == w) ? baddr[(2 * w + 1 < NT) ? 2 * w + 1 : 2 * w] : caddr1;
  }
  const int xrow = wm * 32 + l15;
  const int xa0 = K1_XOFF + xrow * 128 + ((g ^ ((xrow >> 1) & 7)) << 4), xa1 = xa0 ^ 64;
  const int xs0 = xa0 + wn * 2048, xs1 = xa1 + wn * 2048;
  int baddrH[NT], caddr0H = caddr0 + 65536, caddr1H = caddr1 + 65536;
#pragma unroll
  for (int nt = 0; nt < NT; ++nt) { baddrH[nt] = baddr[nt] + 65536; asm volatile("" : "+v"(baddrH[nt])); }
  asm volatile("" : "+v"(caddr0H), "+v"(caddr1H));

  f32x4 acc[2][NT];
#pragma unroll
  for (int mt = 0; mt < 2; ++mt)
#pragma unroll
    for (int nt = 0; nt < NT; ++nt) acc[mt][nt] = f32x4{0.f, 0.f, 0.f, 0.f};
  f32x4 rs_ = {0.f, 0.f, 0.f, 0.f}, rq_ = rs_, cs0 = rs_, cs1 = rs_;
  const u32x4 ONES = {0x3f803f80u, 0x3f803f80u, 0x3f803f80u, 0x3f803f80u};

  struct Frags { u32x4 x0, x1, xs, c0, c1, b[NT]; };
  Frags fr[2];
  auto step = [&](const Frags& f, Frags& n, bool ld, int cn, int ksn, int sc) {
    const int st = cn % 3;
    const int wo = (st == 2 ? 0 : st * K1_WST) + ksn * 8192;
    const int xo = st * K1_XST;
    const int scc = sc < 0 ? 0 : sc;
    const unsigned dx = ldsb + K1_XOFF + (scc % 3) * K1_XST + wave * 1024;
    const unsigned dw = ldsb + (scc % 3) * K1_WST + wave * 1024;
    const char* xb = (const char*)x + scc * 128;
    const char* wb0 = Wh0 + (size_t)scc * 64 * ldw0;
    const char* wb1 = Wh1 + (size_t)scc * 64 * ldw1;
#pragma unroll
    for (int nt = 0; nt < NT; ++nt) {
      if (ld) {
        if (nt == 0) { n.x0 = *(lds_u4*)(lds + (ksn ? xa1 : xa0) + xo); n.x1 = *(lds_u4*)(lds + (ksn ? xa1 : xa0) + xo + 2048); }
        lds_char* bp = lds + (st == 2 ? baddrH[nt] : baddr[nt]) + wo;
        n.b[nt] = tr_read2(bp, bp + 1024);
        if (nt == 3) { lds_char* cp = lds + (st == 2 ? caddr0H : caddr0) + wo; n.c0 = tr_read2(cp, cp + 1024); }
        if (nt == 4) { lds_char* cp = lds + (st == 2 ? caddr1H : caddr1) + wo; n.c1 = tr_read2(cp, cp + 1024); }
        if (nt == 5) n.xs = *(lds_u4*)(lds + (ksn ? xs1 : xs0) + xo);
      }
      const bool trans = (TYPE == T_Q) || (TYPE == T_Z1) || (TYPE == T_KV && nt < 4);
      acc[0][nt] = trans ? mfma16(f.b[nt], f.x0, acc[0][nt]) : mfma16(f.x0, f.b[nt], acc[0][nt]);
      acc[1][nt] = trans ? mfma16(f.b[nt], f.x1, acc[1][nt]) : mfma16(f.x1, f.b[nt], acc[1][nt]);
      if (nt == 1) cs0 = mfma16(ONES, f.c0, cs0);
      if (nt == 2) cs1 = mfma16(ONES, f.c1, cs1);
      if (nt == 3) rs_ = mfma16(ONES, f.xs, rs_);
      if (nt == 4) rq_ = mfma16(f.xs, f.xs, rq_);
      if (sc >= 0 && nt < 6) {
        if (nt < 2) glds16(xb, xoff[nt], dx + nt * 8192);
        else glds16(nt < 4 ? wb0 : wb1, goff[nt - 2], dw + (nt - 2) * 8192);
      }
      __builtin_amdgcn_sched_barrier(0);
    }
  };
  WAIT_VM(12);
  BARRIER();
  {
    Frags& n = fr[0];
    n.x0 = *(lds_u4*)(lds + xa0); n.x1 = *(lds_u4*)(lds + xa0 + 2048);
#pragma unroll
    for (int nt = 0; nt < NT; ++nt) n.b[nt] = tr_read2(lds + baddr[nt], lds + baddr[nt] + 1024);
    n.c0 = tr_read2(lds + caddr0, lds + caddr0 + 1024);
    n.c1 = tr_read2(lds + caddr1, lds + caddr1 + 1024);
    n.xs = *(lds_u4*)(lds + xs0);
  }
#pragma unroll
  for (int c = 0; c < 7; ++c) {
    step(fr[0], fr[1], true, c, 1, -1);
    if (c < 6) {
      if (c < 5) WAIT_VM(6); else WAIT_VM(0);
      WAIT_LGKM0();
      BARRIER();
      if (c == 5 && TYPE == T_KV) {
        unsigned o0 = min(2 * hj * 1568 + wm * 1024 + lane * 16, 25088 - 16);
        glds16(wphi, o0, ldsb + K1_WPHI + wm * 1024);
      }
    }
    step(fr[1], fr[0], c < 6, c + 1, 0, (c + 3 < 7) ? c + 3 : -1);
  }

  lds_f* cstab = (lds_f*)(lds + K1_CS);
  lds_f* rtab = (lds_f*)(lds + K1_RT);
  lds_f* mtab = (lds_f*)(lds + K1_MT);
  {
    float s = rs_[0], c0 = cs0[0], c1 = cs1[0];
    float q = (p4 == 0) ? rq_[0] : (p4 == 1) ? rq_[1] : (p4 == 2) ? rq_[2] : rq_[3];
    q = (g == q4) ? q : 0.f;
    GRP_RED(q, aadd);
    float mean = s * (1.f / 448.f);
    float var = fmaxf(q * (1.f / 448.f) - mean * mean, 0.f);
    float mb = rbf(mean), vb = rbf(var);
    float sd = rbf(__builtin_amdgcn_sqrtf(rbf(vb + 1e-5f)));
    float r = frcp(sd);
    if (g == 0) {
      cstab[(wn * 8 + 2 * wm) * 16 + l15] = c0;
      cstab[(wn * 8 + 2 * wm + 1) * 16 + l15] = c1;
      rtab[wm * 32 + wn * 16 + l15] = r;
      mtab[wm * 32 + wn * 16 + l15] = -mb * r;
    }
  }
  WAIT_VM(0);
  WAIT_LGKM0();
  BARRIER();

  if (TYPE == T_Q || TYPE == T_KV || TYPE == T_Z1) {
    f32x2 Rr[2], Mr[2];
#pragma unroll
    for (int mt = 0; mt < 2; ++mt) {
      float r = rtab[wm * 32 + mt * 16 + l15], m = mtab[wm * 32 + mt * 16 + l15];
      Rr[mt] = f32x2{r, r}; Mr[mt] = f32x2{m, m};
    }
    if (TYPE == T_Z1) {
#pragma unroll
      for (int nt = 0; nt < NT; ++nt) {
        f32x4 c4 = *(lds_f4*)(cstab + (wn * 8 + nt) * 16 + 4 * g);
        const int f = hj * 112 + nt * 16 + 4 * g;
        const int R = (f & 31) >> 3, hh = (f >> 2) & 1;
#pragma unroll
        for (int mt = 0; mt < 2; ++mt) {
          f32x2 a = gelu2(pk_fma(lo2(c4), Mr[mt], lo2(acc[mt][nt]) * Rr[mt]));
          f32x2 b = gelu2(pk_fma(hi2(c4), Mr[mt], hi2(acc[mt][nt]) * Rr[mt]));
          const int s = row0 + wm * 32 + mt * 16 + l15;
          char* dst = ws + WS_Z1B + (size_t)((s >> 5) * 28 + (f >> 5)) * 2048 + R * 512 + ((s & 31) + 32 * hh) * 8;
          st8(dst, u32x2{pack2v(a), pack2v(b)});
        }
      }
      BARRIER();
    } else {
      constexpr int NHW = (TYPE == T_Q) ? 4 : 2;
      const bool inval = (g == 3);
      lds_char* scr = lds + wm * 16384;
      f32x4 pp[2][2][2];
#pragma unroll
      for (int hh = 0; hh < NHW; ++hh) {
        const int head = NHW * hj + hh;
        u32x4 a0, a1;
        if (TYPE == T_KV) {
          lds_char* wp = lds + K1_WPHI + hh * 1568 + (4 * g + q4) * 56 + 8 * p4;
          a0 = tr_read2(wp, wp + 16 * 56);
          a1 = tr_read2(wp + 32, wp + 16 * 56 + 32);
        }
        f32x4 c0 = *(lds_f4*)(cstab + (wn * 8 + 2 * hh) * 16 + 4 * g);
        f32x4 c1 = *(lds_f4*)(cstab + (wn * 8 + 2 * hh + 1) * 16 + 4 * g);
#pragma unroll
        for (int mt = 0; mt < 2; ++mt) {
          u32x4 bq;
          bq[0] = pack2v(pk_fma(lo2(c0), Mr[mt], lo2(acc[mt][2 * hh]) * Rr[mt]));
          bq[1] = pack2v(pk_fma(hi2(c0), Mr[mt], hi2(acc[mt][2 * hh]) * Rr[mt]));
          bq[2] = pack2v(pk_fma(lo2(c1), Mr[mt], lo2(acc[mt][2 * hh + 1]) * Rr[mt]));
          bq[3] = pack2v(pk_fma(hi2(c1), Mr[mt], hi2(acc[mt][2 * hh + 1]) * Rr[mt]));
          bq[2] = inval ? 0u : bq[2]; bq[3] = inval ? 0u : bq[3];
          if (TYPE == T_Q) {
            const int stile = rb * 8 + wm * 2 + mt;
            st16(ws + WS_PHIQ + (size_t)(head * 128 + stile) * 1024 + lane * 16, bq);
          } else {
            f32x4 z4 = {0.f, 0.f, 0.f, 0.f};
            pp[hh & 1][mt][0] = mfma16(a0, bq, z4); pp[hh & 1][mt][1] = mfma16(a1, bq, z4);
          }
        }
      }
      if (TYPE == T_KV) {
        u32x4 vb[2][2];
        f32x4 r4[2], m4[2];
#pragma unroll
        for (int mt = 0; mt < 2; ++mt) {
          r4[mt] = *(lds_f4*)(rtab + wm * 32 + mt * 16 + 4 * g);
          m4[mt] = *(lds_f4*)(mtab + wm * 32 + mt * 16 + 4 * g);
        }
#pragma unroll
        for (int hh = 0; hh < 2; ++hh)
#pragma unroll
          for (int dt = 0; dt < 2; ++dt) {
            const int nt = 4 + 2 * hh + dt;
            float csd = cstab[(wn * 8 + nt) * 16 + l15];
            const f32x2 cc = {csd, csd};
#pragma unroll
            for (int mt = 0; mt < 2; ++mt) {
              vb[hh][dt][2 * mt] = pack2v(pk_fma(cc, lo2(m4[mt]), lo2(acc[mt][nt]) * lo2(r4[mt])));
              vb[hh][dt][2 * mt + 1] = pack2v(pk_fma(cc, hi2(m4[mt]), hi2(acc[mt][nt]) * hi2(r4[mt])));
            }
            if (dt == 1) {
              const unsigned fill = (l15 == 12) ? 0x3f803f80u : 0u;
#pragma unroll
              for (int i = 0; i < 4; ++i) vb[hh][1][i] = (l15 >= 12) ? fill : vb[hh][1][i];
            }
          }
#pragma unroll
        for (int hh = 0; hh < 2; ++hh)
#pragma unroll
          for (int mt = 0; mt < 2; ++mt) {
            u32x4 pos, neg;
            hedgehog_softmax(pp[hh][mt][0], pp[hh][mt][1], inval, pos, neg);
            lds_char* d = scr + hh * 5120 + (mt * 16 + l15) * 160 + 8 * g;
            *(lds_u2*)(d) = u32x2{pos[0], pos[1]};
            *(lds_u2*)(d + 32) = u32x2{pos[2], pos[3]};
            *(lds_u2*)(d + 64) = u32x2{neg[0], neg[1]};
            *(lds_u2*)(d + 96) = u32x2{neg[2], neg[3]};
          }
        f32x4 kv[2][4][2];
#pragma unroll
        for (int hh = 0; hh < 2; ++hh) {
          lds_char* rp = scr + hh * 5120 + (4 * g + q4) * 160 + 8 * p4;
#pragma unroll
          for (int ft = 0; ft < 4; ++ft) {
            u32x4 pa = tr_read2(rp + ft * 32, rp + 16 * 160 + ft * 32);
            f32x4 z4 = {0.f, 0.f, 0.f, 0.f};
            kv[hh][ft][0] = mfma16(pa, vb[hh][0], z4);
            kv[hh][ft][1] = mfma16(pa, vb[hh][1], z4);
          }
        }
        WAIT_LGKM0();
#pragma unroll
        for (int hh = 0; hh < 2; ++hh) {
          lds_char* q = scr + (hh == 0 ? 8192 : 0) + lane * 16;
#pragma unroll
          for (int ft = 0; ft < 4; ++ft) {
            *(lds_f4*)(q + (ft * 2 + 0) * 1024) = kv[hh][ft][0];
            *(lds_f4*)(q + (ft * 2 + 1) * 1024) = kv[hh][ft][1];
          }
        }
      }
      WAIT_LGKM0();
      BARRIER();
      if (TYPE == T_KV) {
#pragma unroll
        for (int it = 0; it < 4; ++it) {
          int pos = it * 256 + tid;
          f32x4 a = *(lds_f4*)(lds + 0 * 16384 + pos * 16);
          a += *(lds_f4*)(lds + 1 * 16384 + pos * 16);
          a += *(lds_f4*)(lds + 2 * 16384 + pos * 16);
          a += *(lds_f4*)(lds + 3 * 16384 + pos * 16);
          int head = 2 * hj + 1 - (pos >> 9);
          st16(ws + WS_KVS + ((size_t)(head * 16 + rb) * 8) * 1024 + (pos & 511) * 16, __builtin_bit_cast(u32x4, a));
        }
      }
    }
  } else {
    lds_f* st = (lds_f*)(lds + K1_ST2);
#pragma unroll
    for (int mt = 0; mt < 2; ++mt) {
      f32x4 r4 = *(lds_f4*)(rtab + wm * 32 + mt * 16 + 4 * g);
      f32x4 m4 = *(lds_f4*)(mtab + wm * 32 + mt * 16 + 4 * g);
      f32x2 sa = {0.f, 0.f}, sb = {0.f, 0.f}, qa = {0.f, 0.f}, qb = {0.f, 0.f};
#pragma unroll
      for (int nt = 0; nt < NT; ++nt) {
        float csd = cstab[(wn * 8 + nt) * 16 + l15];
        const f32x2 cc = {csd, csd};
        f32x2 a = gelu2(pk_fma(cc, lo2(m4), lo2(acc[mt][nt]) * lo2(r4)));
        f32x2 b = gelu2(pk_fma(cc, hi2(m4), hi2(acc[mt][nt]) * hi2(r4)));
        unsigned pa = pack2v(a), pb = pack2v(b);
        sa += a; sb += b; qa = pk_fma(a, a, qa); qb = pk_fma(b, b, qb);
        const int f = hj * 112 + nt * 16 + l15;
        const int s16 = rb * 8 + wm * 2 + mt;
        char* dst = ws + WS_Z2B + (size_t)((f >> 5) * 128 + s16) * 1024 + ((f & 31) + 32 * (g >> 1)) * 16 + (g & 1) * 8;
        st8(dst, u32x2{pa, pb});
      }
      f32x4 o0 = {row_sum16(sa[0]), row_sum16(qa[0]), row_sum16(sa[1]), row_sum16(qa[1])};
      f32x4 o1 = {row_sum16(sb[0]), row_sum16(qb[0]), row_sum16(sb[1]), row_sum16(qb[1])};
      if (l15 == 0) {
        *(lds_f4*)(st + (wn * 128 + wm * 32 + mt * 16 + 4 * g) * 2) = o0;
        *(lds_f4*)(st + (wn * 128 + wm * 32 + mt * 16 + 4 * g) * 2 + 4) = o1;
      }
    }
    WAIT_LGKM0();
    BARRIER();
    if (tid < 128) {
      f32x2 a = *(lds_f2*)(st + tid * 2), b = *(lds_f2*)(st + (128 + tid) * 2);
      *(f32x2*)(ws + WS_STAT + ((size_t)(cg - 10) * 2048 + row0 + tid) * 8) = a + b;
    }
  }
}

__global__ __launch_bounds__(512, 2) void k1_kernel(const __bf16* __restrict__ x, const __bf16* __restrict__ wqkv,
                                                    const __bf16* __restrict__ wphi, const __bf16* __restrict__ win,
                                                    const __bf16* __restrict__ wsp, const __bf16* __restrict__ wo,
                                                    const __bf16* __restrict__ wout, char* __restrict__ ws) {
  lds_char* lds = (lds_char*)smem;
  const int b = blockIdx.x;
  const int xcd = b & 7, idx = b >> 3;
  const int tid = threadIdx.x, lane = tid & 63;
  if (idx >= 28) {
    const int e = xcd * 4 + (idx - 28);
    unsigned acc = 0;
    const u32x4* p = (const u32x4*)wsp + (size_t)e * 16384 + tid;
#pragma unroll 8
    for (int i = 0; i < 32; ++i) { u32x4 v = p[i * 512]; acc ^= v[0] ^ v[3]; }
    const u32x4* q = (const u32x4*)wout + e * 1568 + tid;
#pragma unroll
    for (int i = 0; i < 4; ++i) if (i * 512 + tid < 1568) { u32x4 v = q[i * 512]; acc ^= v[0] ^ v[3]; }
    const u32x4* r = (const u32x4*)wo + e * 784 + tid;
#pragma unroll
    for (int i = 0; i < 2; ++i) if (i * 512 + tid < 784) { u32x4 v = r[i * 512]; acc ^= v[0] ^ v[3]; }
    if (e < 2) {
      unsigned lo, hi;
      if (e == 0)
        asm volatile("s_getpc_b64 s[20:21]\n\ts_add_u32 s20, s20, k2_kernel@rel32@lo+4\n\ts_addc_u32 s21, s21, k2_kernel@rel32@hi+12\n\t"
                     "s_mov_b32 %0, s20\n\ts_mov_b32 %1, s21" : "=s"(lo), "=s"(hi) :: "s20", "s21", "scc");
      else
        asm volatile("s_getpc_b64 s[20:21]\n\ts_add_u32 s20, s20, k3_kernel@rel32@lo+4\n\ts_addc_u32 s21, s21, k3_kernel@rel32@hi+12\n\t"
                     "s_mov_b32 %0, s20\n\ts_mov_b32 %1, s21" : "=s"(lo), "=s"(hi) :: "s20", "s21", "scc");
      const u32x4* c = (const u32x4*)(((unsigned long long)hi << 32) | lo);
      const int n16 = (e == 0 ? K2_CODE_BYTES : K3_CODE_BYTES) / 16;
      for (int i = tid; i < n16; i += 512) { u32x4 v = c[i]; acc ^= v[0] ^ v[3]; }
    }
    asm volatile("" :: "v"(acc));
    return;
  }
  const int rb = 2 * xcd + idx / 14, cg = idx % 14;
  const int wave = __builtin_amdgcn_readfirstlane(tid >> 6);
  const unsigned ldsb = (unsigned)(size_t)lds;
  unsigned goff[4], xoff[2];
#pragma unroll
  for (int i = 0; i < 2; ++i) {
    int row = (i * 8 + wave) * 8 + (lane >> 3);
    int c = (lane & 7) ^ ((row >> 1) & 7);
    xoff[i] = (rb * 128 + row) * 896 + c * 16;
  }
  glds16(x, xoff[0], ldsb + K1_XOFF + wave * 1024);
  glds16(x, xoff[1], ldsb + K1_XOFF + wave * 1024 + 8192);
  int ht[2], hj[2];
  k1_half(cg, 0, ht[0], hj[0]);
  k1_half(cg, 1, ht[1], hj[1]);
  const char* Wh[2]; int ldw[2];
#pragma unroll
  for (int h = 0; h < 2; ++h) {
    const bool qkv = ht[h] <= T_KV;
    Wh[h] = uni_ptr(qkv ? (const char*)wqkv : (const char*)win);
    ldw[h] = __builtin_amdgcn_readfirstlane(qkv ? 2688 : 3584);
    const int colA = (ht[h] == T_KV) ? 896 + 112 * hj[h] : 224 * hj[h] + (ht[h] == T_Z2 ? 1792 : 0);
    const int delta = (ht[h] == T_KV) ? 784 : 0, lim = (ht[h] == T_Q) ? 15 : 14;
#pragma unroll
    for (int i = 0; i < 2; ++i) {
      int row = (i * 8 + wave) * 4 + (lane >> 4);
      int sw = (row & 3) | (((row >> 3) & 1) << 2);
      int c = (lane & 15) ^ (sw << 1);
      c = c < lim ? c : 0;
      goff[h * 2 + i] = row * ldw[h] + colA + 16 * c + (c >= 7 ? delta : 0);
    }
  }
#pragma unroll
  for (int chunk = 0; chunk < 3; ++chunk) {
    const unsigned dx = ldsb + K1_XOFF + chunk * K1_XST + wave * 1024;
    const unsigned dw = ldsb + chunk * K1_WST + wave * 1024;
    const char* xb = (const char*)x + chunk * 128;
    if (chunk > 0) {
      glds16(xb, xoff[0], dx);
      glds16(xb, xoff[1], dx + 8192);
    }
#pragma unroll
    for (int i = 0; i < 4; ++i) glds16(Wh[i >> 1] + (size_t)chunk * 64 * ldw[i >> 1], goff[i], dw + i * 8192);
  }
  const int my = (wave >> 2) ? ht[1] : ht[0], mj = (wave >> 2) ? hj[1] : hj[0];
  if (my == T_Q) k1_wave<T_Q>(x, Wh[0], Wh[1], ldw[0], ldw[1], goff, xoff, wphi, ws, lds, rb, cg, mj);
  else if (my == T_KV) k1_wave<T_KV>(x, Wh[0], Wh[1], ldw[0], ldw[1], goff, xoff, wphi, ws, lds, rb, cg, mj);
  else if (my == T_Z1) k1_wave<T_Z1>(x, Wh[0], Wh[1], ldw[0], ldw[1], goff, xoff, wphi, ws, lds, rb, cg, mj);
  else k1_wave<T_Z2>(x, Wh[0], Wh[1], ldw[0], ldw[1], goff, xoff, wphi, ws, lds, rb, cg, mj);
}

constexpr int K2_WB = 0;
constexpr int K2_RT = 32768;
constexpr int K2_MT = K2_RT + 8192;
constexpr int K2_CT = K2_MT + 4096;
constexpr int K2_LDS = K2_CT + 512;

extern "C" __global__ __launch_bounds__(768, 3) void k2_kernel(const __bf16* __restrict__ wsp, const __bf16* __restrict__ bsp,
                                                    const __bf16* __restrict__ wphi, char* __restrict__ ws) {
  lds_char* lds = (lds_char*)smem;
  const int tid = threadIdx.x, lane = tid & 63;
  const int wave = __builtin_amdgcn_readfirstlane(tid >> 6);
  const int b = blockIdx.x, xcd = b & 7, idx = b >> 3;
  if (idx >= 28) {
    if (wave >= 8) return;
    const int e = xcd * 4 + (idx - 28), head = e >> 1, half = e & 1;
    const int g = lane >> 4, l15 = lane & 15, q4 = (lane >> 2) & 3, p4 = lane & 3;
    u32x4 qf[8];
    const char* pbase = ws + WS_PHIQ + (size_t)(head * 128 + half * 64 + wave * 8) * 1024 + lane * 16;
#pragma unroll
    for (int i = 0; i < 8; ++i) qf[i] = *(const u32x4*)(pbase + i * 1024);
    if (tid < 128) {
      const unsigned o = min(head * 1568 + tid * 16, 25088 - 16);
      *(lds_u4*)(lds + 8192 + tid * 16) = *(const u32x4*)((const char*)wphi + o);
    }
    {
      const char* kb = ws + WS_KVS + ((size_t)(head * 16) * 8 + wave) * 1024 + lane * 16;
      f32x4 a = {0.f, 0.f, 0.f, 0.f};
#pragma unroll
      for (int r = 0; r < 16; ++r) a += *(const f32x4*)(kb + (size_t)r * 8192);
      *(lds_u2*)(lds + wave * 512 + lane * 8) = u32x2{pack2(a[0], a[1]), pack2(a[2], a[3])};
    }
    __syncthreads();
    u32x4 af[2][2];
#pragma unroll
    for (int sg = 0; sg < 2; ++sg)
#pragma unroll
      for (int dt = 0; dt < 2; ++dt) {
        u32x2 lo = *(lds_u2*)(lds + ((2 * sg) * 2 + dt) * 512 + lane * 8);
        u32x2 hi = *(lds_u2*)(lds + ((2 * sg + 1) * 2 + dt) * 512 + lane * 8);
        af[sg][dt] = u32x4{lo[0], lo[1], hi[0], hi[1]};
      }
    lds_char* wp = lds + 8192 + (4 * g + q4) * 56 + 8 * p4;
    const u32x4 a0 = tr_read2(wp, wp + 16 * 56), a1 = tr_read2(wp + 32, wp + 16 * 56 + 32);
    const bool inval = (g == 3);
#pragma unroll
    for (int i = 0; i < 8; ++i) {
      f32x4 z4 = {0.f, 0.f, 0.f, 0.f};
      f32x4 p0 = mfma16(a0, qf[i], z4), p1 = mfma16(a1, qf[i], z4);
      u32x4 pos, neg;
      hedgehog_softmax(p0, p1, inval, pos, neg);
      f32x4 n0 = mfma16(af[0][0], pos, z4); n0 = mfma16(af[1][0], neg, n0);
      f32x4 n1 = mfma16(af[0][1], pos, z4); n1 = mfma16(af[1][1], neg, n1);
      float den = __shfl(n1[0], 48 + l15);
      den = rbf(rbf(den) + 1e-6f);
      float rd = 1.f / den;
      const int s = half * 1024 + (wave * 8 + i) * 16 + l15;
#pragma unroll
      for (int dt = 0; dt < 2; ++dt) {
        const f32x4& n = dt ? n1 : n0;
        u32x2 o = {pack2(rbf(n[0]) * rd, rbf(n[1]) * rd), pack2(rbf(n[2]) * rd, rbf(n[3]) * rd)};
        const int k = 28 * head + 16 * dt + 4 * g;
        const int kl = k & 15, hh = (kl >> 2) & 1, j0 = (kl >> 3) * 4;
        char* dst = ws + WS_HO + (size_t)((s >> 5) * 84 + (k >> 4)) * 1024 + ((s & 31) + 32 * hh) * 16 + j0 * 2;
        if (!(dt == 1 && g == 3)) st8(dst, o);
      }
    }
    return;
  }

  const int rb = 4 * xcd + idx / 7, ct = idx % 7;
  constexpr int NK = 16;
  const int koff = (ct * 2 + (ct > 3 ? 1 : 0)) & 15;
#define KROT(k) ((((k) + koff) & 15))
  auto stat_load = [&](int row, f32x2 (&a)[4]) {
#pragma unroll
    for (int gq = 0; gq < 4; ++gq) a[gq] = *(const f32x2*)(ws + WS_STAT + ((size_t)gq * 2048 + row) * 8);
  };
  auto stat_put = [&](int row, const f32x2 (&a)[4]) {
    f32x2 t = (a[0] + a[1]) + (a[2] + a[3]);
    float mean = t[0] * (1.f / 896.f);
    float var = fmaxf(t[1] * (1.f / 896.f) - mean * mean, 0.f);
    float vb = rbf(var);
    float sd = rbf(__builtin_amdgcn_sqrtf(rbf(vb + 1e-5f)));
    *(lds_f*)(lds + K2_RT + row * 4) = frcp(sd);
    *(__attribute__((address_space(3))) unsigned short*)(lds + K2_MT + row * 2) = (unsigned short)pack2(mean, 0.f);
  };
  const int srow = KROT(tid >> 7) * 128 + (tid & 127);
  f32x2 sa0[4];
  if (wave >= 4) {
    __builtin_amdgcn_s_setprio(2);
    const int pt = tid - 256, t = pt >> 4, cp = pt & 15;
    const int g = lane >> 4, l15 = lane & 15;
    const char* src = (const char*)wsp + (size_t)(rb * 64 + t) * 4096 + cp * 16;
    constexpr int PD = 4;
    u32x4 wr[PD][2];
#define WLOAD(dst, kk) do { _Pragma("unroll") for (int j = 0; j < 2; ++j) \
      dst[j] = *(const u32x4*)(src + KROT(kk) * 256 + j * (32 * 4096)); } while (0)
    WLOAD(wr[0], 0);
    if (tid < 512) {
      stat_load(srow, sa0);
      __builtin_amdgcn_sched_barrier(0);
      stat_put(srow, sa0);
    }
    const int wa = t * 256 + ((cp ^ (t & 15)) << 4);
    const int ta = cp * 32;
    const int pw = wave - 4;
    const int crow = (pw >> 1) * 16 + l15;
    const int ca = (crow * 256 + ((g ^ l15) << 4)) ^ ((pw & 1) << 7);
    f32x4 cacc = {0.f, 0.f, 0.f, 0.f};
    WAIT_LGKM0(); BARRIER();
    f32x4 rr[2][2];
    rr[0][0] = *(lds_f4*)(lds + K2_RT + KROT(0) * 512 + ta); rr[0][1] = *(lds_f4*)(lds + K2_RT + KROT(0) * 512 + ta + 16);
    WLOAD(wr[1], 1);
    f32x2 sb[3][4];
    __builtin_amdgcn_sched_barrier(0);
#pragma unroll
    for (int k = 0; k <= NK; ++k) {
      if (k == 1) {
#pragma unroll
        for (int i = 0; i < 3; ++i) stat_load(KROT(4 + 4 * i + (pt >> 7)) * 128 + (pt & 127), sb[i]);
      }
      lds_char* wb = lds + K2_WB + (k & 1) * 16384;
      lds_char* pb = lds + K2_WB + ((k + 1) & 1) * 16384;
      u32x4 mf[2], wf[2];
#pragma unroll
      for (int j = 0; j < 2; ++j) {
        if (k > 0) {
          mf[j] = *(lds_u4*)(lds + K2_MT + (KROT(k - 1) * 128 + ((pw & 1) * 2 + j) * 32 + g * 8) * 2);
          wf[j] = *(lds_u4*)(pb + (ca ^ (j << 6)));
        }
      }
      if (k + 1 < NK) {
        rr[(k + 1) & 1][0] = *(lds_f4*)(lds + K2_RT + KROT(k + 1) * 512 + ta);
        rr[(k + 1) & 1][1] = *(lds_f4*)(lds + K2_RT + KROT(k + 1) * 512 + ta + 16);
      }
      __builtin_amdgcn_sched_barrier(0);
#pragma unroll
      for (int j = 0; j < 2; ++j) {
        if (k < NK) {
          const f32x4 r0 = rr[k & 1][0], r1 = rr[k & 1][1];
          u32x4 w = wr[k % PD][j];
          if (k + PD < NK) wr[k % PD][j] = *(const u32x4*)(src + KROT(k + PD) * 256 + j * (32 * 4096));
          u32x4 o;
          o[0] = pack2(smul(bf_lo(w[0]), r0[0]), smul(bf_hi(w[0]), r0[1]));
          o[1] = pack2(smul(bf_lo(w[1]), r0[2]), smul(bf_hi(w[1]), r0[3]));
          o[2] = pack2(smul(bf_lo(w[2]), r1[0]), smul(bf_hi(w[2]), r1[1]));
          o[3] = pack2(smul(bf_lo(w[3]), r1[2]), smul(bf_hi(w[3]), r1[3]));
          *(lds_u4*)(wb + wa + j * 8192) = o;
        }
        __builtin_amdgcn_sched_barrier(0);
        if (k > 0) cacc = mfma16(mf[j], wf[j], cacc);
      }
      if (k == 0) { WLOAD(wr[2], 2); WLOAD(wr[3], 3); }
      if (k == 2) {
#pragma unroll
        for (int i = 0; i < 3; ++i) stat_put(KROT(4 + 4 * i + (pt >> 7)) * 128 + (pt & 127), sb[i]);
      }
      if (k == NK) {
        if (g == 0) *(lds_f*)(lds + K2_CT + ((pw & 1) * 64 + crow) * 4) = cacc[0];
      }
      WAIT_LGKM0();
      BARRIER();
    }
  } else {
    const int F = 4 * ct + wave, l31 = lane & 31, hh = lane >> 5;
    const char* zsrc = ws + WS_Z2B + (size_t)F * 128 * 1024 + lane * 16;
    constexpr int ZD = 16;
    u32x4 zf[ZD];
#pragma unroll
    for (int i = 0; i < 8; ++i) zf[i] = *(const u32x4*)(zsrc + (KROT(i >> 3) * 8 + (i & 7)) * 1024);
    stat_load(srow, sa0);
    __builtin_amdgcn_sched_barrier(0);
    stat_put(srow, sa0);
    WAIT_LGKM0(); BARRIER();
    const int ra = l31 * 256 + ((hh ^ (l31 & 15)) << 4);
    f32x16 acc[2];
#pragma unroll
    for (int i = 0; i < 16; ++i) { acc[0][i] = 0.f; acc[1][i] = 0.f; }
    u32x2 z1v[2][4];
    float bb[2];
    BARRIER();
    int rk[8];
#pragma unroll
    for (int kk = 0; kk < 8; ++kk) { rk[kk] = ra ^ (kk << 5); }
    constexpr int LD = 3;
    u32x4 wq[LD + 1][2];
#pragma unroll
    for (int i = 0; i < LD; ++i) { wq[i][0] = *(lds_u4*)(lds + rk[i]); wq[i][1] = *(lds_u4*)(lds + rk[i] + 8192); }
#pragma unroll
    for (int k = 0; k < NK; ++k) {
#pragma unroll
      for (int kk = 0; kk < 8; ++kk) {
        const int ks = k * 8 + kk;
        if (kk == 8 - LD && k + 1 < NK) { WAIT_LGKM0(); BARRIER(); }
        if (ks + LD < 128) {
          const int nb = (((ks + LD) >> 3) & 1) * 16384;
          wq[(ks + LD) % (LD + 1)][0] = *(lds_u4*)(lds + rk[(kk + LD) & 7] + nb);
          wq[(ks + LD) % (LD + 1)][1] = *(lds_u4*)(lds + rk[(kk + LD) & 7] + nb + 8192);
        }
        const u32x4 w0 = wq[ks % (LD + 1)][0], w1 = wq[ks % (LD + 1)][1];
        if (ks == 108) {
#pragma unroll
          for (int tt = 0; tt < 2; ++tt) {
            const int t = rb * 64 + tt * 32 + l31;
            bb[tt] = bf_lo((unsigned)*(const unsigned short*)(bsp + t));
            const char* z1p = ws + WS_Z1B + (size_t)((t >> 5) * 28 + F) * 2048 + lane * 8;
#pragma unroll
            for (int R = 0; R < 4; ++R) z1v[tt][R] = *(const u32x2*)(z1p + R * 512);
          }
        }
        u32x4 z = zf[ks % ZD];
#define ZLOAD(i) zf[(i) % ZD] = *(const u32x4*)(zsrc + (KROT((i) >> 3) * 8 + ((i) & 7)) * 1024)
        if (ks < ZD - 8) { ZLOAD(8 + 2 * ks); ZLOAD(9 + 2 * ks); }
        else if (ks + ZD < 128) ZLOAD(ks + ZD);
        acc[0] = mfma32(z, w0, acc[0]);
        acc[1] = mfma32(z, w1, acc[1]);
        __builtin_amdgcn_sched_barrier(0);
      }
    }
    WAIT_LGKM0();
    BARRIER();
#pragma unroll
    for (int tt = 0; tt < 2; ++tt) {
      const int t = rb * 64 + tt * 32 + l31;
      const float c = *(lds_f*)(lds + K2_CT + (tt * 32 + l31) * 4) + *(lds_f*)(lds + K2_CT + (64 + tt * 32 + l31) * 4);
      const f32x2 cc = {c, c}, b2 = {bb[tt], bb[tt]};
      unsigned hp[8];
#pragma unroll
      for (int R = 0; R < 4; ++R) {
        f32x2 g0 = unpack2(pack2v(f32x2{acc[tt][4 * R + 0], acc[tt][4 * R + 1]} - cc));
        f32x2 g1 = unpack2(pack2v(f32x2{acc[tt][4 * R + 2], acc[tt][4 * R + 3]} - cc));
        g0 = unpack2(pack2v(g0 + b2)); g1 = unpack2(pack2v(g1 + b2));
        hp[2 * R] = pack2v(unpack2(z1v[tt][R][0]) * g0); hp[2 * R + 1] = pack2v(unpack2(z1v[tt][R][1]) * g1);
      }
      char* dst = ws + WS_HO + (size_t)((t >> 5) * 84 + 28 + 2 * F) * 1024 + lane * 16;
      st16(dst, u32x4{hp[0], hp[1], hp[2], hp[3]});
      st16(dst + 1024, u32x4{hp[4], hp[5], hp[6], hp[7]});
    }
  }
}

constexpr int K3_STG = 16384;
constexpr int K3_NB = 5;
constexpr int K3_LDS = K3_STG * K3_NB;

extern "C" __global__ __launch_bounds__(256, 2) void k3_kernel(const __bf16* __restrict__ x, const __bf16* __restrict__ wo,
                                                    const __bf16* __restrict__ wout, char* __restrict__ ws,
                                                    __bf16* __restrict__ out) {
  lds_char* lds = (lds_char*)smem;
  const unsigned ldsb = (unsigned)(size_t)lds;
  const int tid = threadIdx.x, lane = tid & 63;
  const int wave = __builtin_amdgcn_readfirstlane(tid >> 6);
  const int b = blockIdx.x, xcd = b & 7, idx = b >> 3;
  const int rb = 4 * xcd + idx / 7, ct = idx % 7;
  const int wt = wave & 1, wn = wave >> 1;
  const int l31 = lane & 31, hh = lane >> 5, gi = lane >> 4, q4 = (lane >> 2) & 3, p4 = lane & 3;

  unsigned goff[2];
#pragma unroll
  for (int i = 0; i < 2; ++i) {
    int row = (i * 4 + wave) * 8 + (lane >> 3);
    int sw = ((row & 3) << 1) ^ (((row >> 3) & 1));
    int c = (lane & 7) ^ sw;
    goff[i] = row * 896 + ct * 128 + c * 16;
  }
  const char* abase = ws + WS_HO + (size_t)(rb * 2) * 84 * 1024;
  const unsigned aoff = wave * 1024 + lane * 16;
  auto stage = [&](int chunk) {
    const char* base = (chunk < 7) ? ((const char*)wo + (size_t)chunk * 64 * 896) : ((const char*)wout + (size_t)(chunk - 7) * 64 * 896);
    const unsigned dst = ldsb + (chunk % K3_NB) * K3_STG + wave * 1024;
    glds16(abase + chunk * 4096, aoff, dst);
    glds16(abase + chunk * 4096 + 84 * 1024, aoff, dst + 4096);
    glds16(base, goff[0], dst + 8192);
    glds16(base, goff[1], dst + 8192 + 4096);
  };
  const int t = rb * 64 + wt * 32 + l31;
  u32x2 xv[4];
  {
    const unsigned xo = (unsigned)(t * D_ + ct * 64 + wn * 32 + 4 * hh) * 2;
    asm volatile("global_load_dwordx2 %0, %4, %5\n\tglobal_load_dwordx2 %1, %4, %5 offset:16\n\t"
                 "global_load_dwordx2 %2, %4, %5 offset:32\n\tglobal_load_dwordx2 %3, %4, %5 offset:48"
                 : "=&v"(xv[0]), "=&v"(xv[1]), "=&v"(xv[2]), "=&v"(xv[3]) : "v"(xo), "s"(x) : "memory");
  }
  stage(0); stage(1); stage(2); stage(3);

  int baddr;
  {
    int row = 4 * (gi >> 1) + q4;
    int byte = 2 * (wn * 32 + (gi & 1) * 16 + 4 * p4);
    int sw = ((row & 3) << 1);
    baddr = 8192 + row * 128 + (((byte >> 4) ^ sw) << 4) + (byte & 15);
  }
  const int baddr2 = (baddr ^ 16) + 8 * 128;
  const int aaddr = wt * 4096 + lane * 16;

  f32x16 acc;
#pragma unroll
  for (int i = 0; i < 16; ++i) acc[i] = 0.f;
  unsigned t1[8];
  asm volatile("s_waitcnt vmcnt(12)" : "+v"(xv[0]), "+v"(xv[1]), "+v"(xv[2]), "+v"(xv[3]) :: "memory");
  BARRIER();
  u32x4 wf = tr_read2(lds + baddr, lds + baddr2), af = *(lds_u4*)(lds + aaddr);
#pragma unroll
  for (int c = 0; c < 21; ++c) {
#pragma unroll
    for (int kk = 0; kk < 4; ++kk) {
      u32x4 wn_ = wf, an_ = af;
      if (kk == 3 && c + 1 < 21) {
        if (c + 3 < 21) WAIT_VM(8); else if (c + 2 < 21) WAIT_VM(4); else WAIT_VM(0);
        WAIT_LGKM0();
        BARRIER();
        if (c + 4 < 21) stage(c + 4);
      }
      if (c * 4 + kk + 1 < 84) {
        const int cn = (kk == 3) ? c + 1 : c, kn = (kk + 1) & 3;
        lds_char* bufp = lds + (cn % K3_NB) * K3_STG;
        wn_ = tr_read2(bufp + baddr + kn * 2048, bufp + baddr2 + kn * 2048);
        an_ = *(lds_u4*)(bufp + aaddr + kn * 1024);
      }
      acc = mfma32(wf, af, acc);
      wf = wn_; af = an_;
      __builtin_amdgcn_sched_barrier(0);
    }
    if (c == 6) {
#pragma unroll
      for (int R = 0; R < 4; ++R) {
        f32x2 a0 = unpack2(pack2v(f32x2{acc[4 * R + 0], acc[4 * R + 1]}));
        f32x2 a1 = unpack2(pack2v(f32x2{acc[4 * R + 2], acc[4 * R + 3]}));
        t1[2 * R] = pack2v(unpack2(xv[R][0]) + a0); t1[2 * R + 1] = pack2v(unpack2(xv[R][1]) + a1);
      }
#pragma unroll
      for (int i = 0; i < 16; ++i) acc[i] = 0.f;
    }
  }
  __bf16* orow = out + (size_t)t * D_ + ct * 64 + wn * 32 + 4 * hh;
#pragma unroll
  for (int R = 0; R < 4; ++R) {
    f32x2 a0 = unpack2(pack2v(f32x2{acc[4 * R + 0], acc[4 * R + 1]}));
    f32x2 a1 = unpack2(pack2v(f32x2{acc[4 * R + 2], acc[4 * R + 3]}));
    *(u32x2*)(orow + 8 * R) = u32x2{pack2v(unpack2(t1[2 * R]) + a0), pack2v(unpack2(t1[2 * R + 1]) + a1)};
  }
}

extern "C" void kernel_launch(void* const* d_in, const int* in_sizes, int n_in, void* d_out, int out_size,
                              void* d_ws, size_t ws_size, hipStream_t stream) {
  const __bf16* x = (const __bf16*)d_in[0];
  const __bf16* wqkv = (const __bf16*)d_in[1];
  const __bf16* wphi = (const __bf16*)d_in[2];
  const __bf16* wo = (const __bf16*)d_in[3];
  const __bf16* win = (const __bf16*)d_in[5];
  const __bf16* wsp = (const __bf16*)d_in[6];
  const __bf16* bsp = (const __bf16*)d_in[7];
  const __bf16* wout = (const __bf16*)d_in[8];
  char* ws = (char*)d_ws;
  (void)hipFuncSetAttribute((const void*)k1_kernel, hipFuncAttributeMaxDynamicSharedMemorySize, K1_LDS);
  (void)hipFuncSetAttribute((const void*)k3_kernel, hipFuncAttributeMaxDynamicSharedMemorySize, K3_LDS);
  k1_kernel<<<256, 512, K1_LDS, stream>>>(x, wqkv, wphi, win, wsp, wo, wout, ws);
  k2_kernel<<<256, 768, K2_LDS, stream>>>(wsp, bsp, wphi, ws);
  k3_kernel<<<224, 256, K3_LDS, stream>>>(x, wo, wout, ws, (__bf16*)d_out);
}
```

```cpp
#include <hip/hip_runtime.h>
#include <hip/hip_bf16.h>
#include <stdint.h>

#define DI __device__ __forceinline__
typedef __attribute__((ext_vector_type(8))) __bf16 bf16x8;
typedef __attribute__((ext_vector_type(2))) __bf16 bf16x2;
typedef __attribute__((ext_vector_type(4))) short s16x4;
typedef __attribute__((ext_vector_type(4))) float f32x4;
typedef __attribute__((ext_vector_type(2))) float f32x2;
typedef __attribute__((ext_vector_type(16))) float f32x16;
typedef __attribute__((ext_vector_type(4))) unsigned u32x4;
typedef __attribute__((ext_vector_type(2))) unsigned u32x2;
typedef __attribute__((address_space(3))) char lds_char;
typedef __attribute__((address_space(3))) s16x4 lds_s16x4;

constexpr int S_ = 2048, D_ = 448;

constexpr size_t WS_PHIQ = 0;
constexpr size_t WS_KVS = 4u << 20;
constexpr size_t WS_Z1B = 6u << 20;
constexpr size_t WS_Z2B = 10u << 20;
constexpr size_t WS_STAT = 14u << 20;
constexpr size_t WS_HO = 15u << 20;

DI unsigned pack2(float a, float b) {
  f32x2 v = {a, b};
  return __builtin_bit_cast(unsigned, __builtin_convertvector(v, bf16x2));
}
DI float bf_lo(unsigned u) { return __uint_as_float(u << 16); }
DI float bf_hi(unsigned u) { return __uint_as_float(u & 0xffff0000u); }
DI float rbf(float x) { return bf_lo(pack2(x, 0.f)); }
DI f32x4 mfma16(u32x4 a, u32x4 b, f32x4 c) {
  return __builtin_amdgcn_mfma_f32_16x16x32_bf16(__builtin_bit_cast(bf16x8, a), __builtin_bit_cast(bf16x8, b), c, 0, 0, 0);
}
DI f32x16 mfma32(u32x4 a, u32x4 b, f32x16 c) {
  return __builtin_amdgcn_mfma_f32_32x32x16_bf16(__builtin_bit_cast(bf16x8, a), __builtin_bit_cast(bf16x8, b), c, 0, 0, 0);
}
DI u32x2 tr_read(lds_char* p) {
  s16x4 r = __builtin_amdgcn_ds_read_tr16_b64_v4i16((lds_s16x4*)p);
  return __builtin_bit_cast(u32x2, r);
}
DI u32x4 tr_read2(lds_char* p0, lds_char* p1) {
  u32x2 a = tr_read(p0), b = tr_read(p1);
  u32x4 r = {a[0], a[1], b[0], b[1]};
  return r;
}
DI float frcp(float x) { return __builtin_amdgcn_rcpf(x); }
DI const char* uni_ptr(const char* p) {
  unsigned long long v = (unsigned long long)p;
  unsigned lo = __builtin_amdgcn_readfirstlane((unsigned)v), hi = __builtin_amdgcn_readfirstlane((unsigned)(v >> 32));
  return (const char*)(((unsigned long long)hi << 32) | lo);
}
DI void glds16(const void* sbase, unsigned voff, unsigned lds_dst) {
  asm volatile("s_mov_b32 m0, %2\n\ts_nop 3\n\tglobal_load_lds_dwordx4 %0, %1"
               :: "v"(voff), "s"(sbase), "s"(lds_dst) : "memory");
}
DI void st16(void* p, u32x4 v) {
  asm volatile("global_store_dwordx4 %0, %1, off sc0 sc1\n\ts_nop 1" :: "v"(p), "v"(v) : "memory");
}
DI void st8(void* p, u32x2 v) {
  asm volatile("global_store_dwordx2 %0, %1, off sc0 sc1\n\ts_nop 1" :: "v"(p), "v"(v) : "memory");
}
#define WAIT_VMI(n) asm volatile("s_waitcnt vmcnt(%0)" :: "i"(n) : "memory")
#define WAIT_VM(n) asm volatile("s_waitcnt vmcnt(" #n ")" ::: "memory")
#define WAIT_LGKM0() asm volatile("s_waitcnt lgkmcnt(0)" ::: "memory")
#define BARRIER() __builtin_amdgcn_s_barrier()

constexpr int K1_WST = 32768;
constexpr int K1_XOFF = 98304;
constexpr int K1_XST = 16384;
constexpr int K1_WPHI = 131072;
constexpr int K1_CS = K1_WPHI + 13312;
constexpr int K1_RT = K1_CS + 1024;
constexpr int K1_MT = K1_RT + 512;
constexpr int K1_ST2 = K1_MT + 512;
constexpr int K1_LDS = 147456 + 2048;

enum { T_Q = 0, T_KV = 1, T_Z1 = 2, T_Z2 = 3 };

typedef __attribute__((address_space(3))) float lds_f;
typedef __attribute__((address_space(3))) f32x4 lds_f4;
typedef __attribute__((address_space(3))) f32x2 lds_f2;
typedef __attribute__((address_space(3))) u32x4 lds_u4;
typedef __attribute__((address_space(3))) u32x2 lds_u2;

DI float amax(float a, float b) { float r; asm("v_max_f32 %0, %1, %2" : "=v"(r) : "v"(a), "v"(b)); return r; }
DI float amin(float a, float b) { float r; asm("v_min_f32 %0, %1, %2" : "=v"(r) : "v"(a), "v"(b)); return r; }
#define GRP_RED(x, OP) do { \
    auto r_ = __builtin_amdgcn_permlane16_swap(__float_as_uint(x), __float_as_uint(x), false, false); \
    x = OP(__uint_as_float(r_[0]), __uint_as_float(r_[1])); \
    auto q_ = __builtin_amdgcn_permlane32_swap(__float_as_uint(x), __float_as_uint(x), false, false); \
    x = OP(__uint_as_float(q_[0]), __uint_as_float(q_[1])); } while (0)
DI float aadd(float a, float b) { return a + b; }
DI float smul(float a, float b) { float r; asm("v_mul_f32 %0, %1, %2" : "=v"(r) : "v"(a), "v"(b)); return r; }
DI f32x2 pk_fma(f32x2 a, f32x2 b, f32x2 c) { return __builtin_elementwise_fma(a, b, c); }
DI f32x2 lo2(const f32x4& v) { return f32x2{v[0], v[1]}; }
DI f32x2 hi2(const f32x4& v) { return f32x2{v[2], v[3]}; }
DI unsigned pack2v(f32x2 v) { return __builtin_bit_cast(unsigned, __builtin_convertvector(v, bf16x2)); }
DI f32x2 unpack2(unsigned u) { return f32x2{bf_lo(u), bf_hi(u)}; }
DI f32x2 gelu2(f32x2 v) {
  const f32x2 c1 = {-2.302208198f, -2.302208198f};
  const f32x2 c2 = {-0.1029432397f, -0.1029432397f};
  f32x2 u = v * pk_fma(v * v, c2, c1);
  f32x2 d = f32x2{__builtin_amdgcn_exp2f(u[0]), __builtin_amdgcn_exp2f(u[1])} + f32x2{1.f, 1.f};
  return v * f32x2{frcp(d[0]), frcp(d[1])};
}
DI float row_sum16(float x) {
  x += __builtin_bit_cast(float, __builtin_amdgcn_mov_dpp(__builtin_bit_cast(int, x), 0x128, 0xf, 0xf, true));
  x += __builtin_bit_cast(float, __builtin_amdgcn_mov_dpp(__builtin_bit_cast(int, x), 0x124, 0xf, 0xf, true));
  x += __builtin_bit_cast(float, __builtin_amdgcn_mov_dpp(__builtin_bit_cast(int, x), 0x122, 0xf, 0xf, true));
  x += __builtin_bit_cast(float, __builtin_amdgcn_mov_dpp(__builtin_bit_cast(int, x), 0x121, 0xf, 0xf, true));
  return x;
}

DI void hedgehog_softmax(const f32x4& p0, const f32x4& p1, bool inval, u32x4& pos, u32x4& neg) {
  f32x2 v[4];
  v[0] = unpack2(pack2v(lo2(p0))); v[1] = unpack2(pack2v(hi2(p0)));
  v[2] = unpack2(pack2v(lo2(p1))); v[3] = unpack2(pack2v(hi2(p1)));
  f32x2 w2 = inval ? v[0] : v[2], w3 = inval ? v[1] : v[3];
  float mx = __builtin_fmaxf(__builtin_fmaxf(__builtin_fmaxf(v[0][0], v[0][1]), __builtin_fmaxf(v[1][0], v[1][1])),
                             __builtin_fmaxf(__builtin_fmaxf(w2[0], w2[1]), __builtin_fmaxf(w3[0], w3[1])));
  float mn = __builtin_fminf(__builtin_fminf(__builtin_fminf(v[0][0], v[0][1]), __builtin_fminf(v[1][0], v[1][1])),
                             __builtin_fminf(__builtin_fminf(w2[0], w2[1]), __builtin_fminf(w3[0], w3[1])));
  GRP_RED(mx, amax);
  GRP_RED(mn, amin);
  const float L = 1.4426950408889634f;
  const f32x2 L2 = {L, L}, nL2 = {-L, -L};
  const f32x2 bx = {-mx * L, -mx * L}, bn = {mn * L, mn * L};
  f32x2 e1[4], e2[4];
#pragma unroll
  for (int i = 0; i < 4; ++i) {
    f32x2 a = pk_fma(v[i], L2, bx), b = pk_fma(v[i], nL2, bn);
    e1[i] = f32x2{__builtin_amdgcn_exp2f(a[0]), __builtin_amdgcn_exp2f(a[1])};
    e2[i] = f32x2{__builtin_amdgcn_exp2f(b[0]), __builtin_amdgcn_exp2f(b[1])};
  }
  const f32x2 zz = {0.f, 0.f};
  e1[2] = inval ? zz : e1[2]; e1[3] = inval ? zz : e1[3];
  e2[2] = inval ? zz : e2[2]; e2[3] = inval ? zz : e2[3];
  f32x2 t1 = (e1[0] + e1[1]) + (e1[2] + e1[3]), t2 = (e2[0] + e2[1]) + (e2[2] + e2[3]);
  float s1 = t1[0] + t1[1], s2 = t2[0] + t2[1];
  GRP_RED(s1, aadd);
  GRP_RED(s2, aadd);
  float r1 = frcp(s1), r2 = frcp(s2);
  const f32x2 R1 = {r1, r1}, R2 = {r2, r2};
#pragma unroll
  for (int i = 0; i < 4; ++i) { pos[i] = pack2v(e1[i] * R1); neg[i] = pack2v(e2[i] * R2); }
}

DI void k1_half(int cg, int wn, int& ht, int& j) {
  const int lo = (wn == 0) ? T_KV : (cg < 4 ? T_Q : T_Z1);
  ht = cg < 8 ? lo : (cg < 10 ? T_Z1 : T_Z2);
  const int jlo = (wn == 0 || cg < 4) ? cg : cg - 4;
  j = cg < 8 ? jlo : (cg < 10 ? 2 * cg - 12 + wn : 2 * cg - 20 + wn);
}

template <int TYPE>
DI void k1_wave(const __bf16* __restrict__ x, const char* Wh0, const char* Wh1, int ldw0, int ldw1,
                const unsigned (&goff)[4], const unsigned (&xoff)[2], const __bf16* __restrict__ wphi,
                char* __restrict__ ws, lds_char* lds, int rb, int cg, int hj) {
  constexpr int NT = (TYPE == T_Q || TYPE == T_KV) ? 8 : 7;
  const int tid = threadIdx.x, lane = tid & 63;
  const int wave = __builtin_amdgcn_readfirstlane(tid >> 6);
  const int wm = wave & 3, wn = wave >> 2;
  const int g = lane >> 4, l15 = lane & 15, q4 = (lane >> 2) & 3, p4 = lane & 3;
  const int row0 = rb * 128;
  const unsigned ldsb = (unsigned)(size_t)lds;

  int baddr[NT];
#pragma unroll
  for (int nt = 0; nt < NT; ++nt) {
    int n0;
    if (TYPE == T_Q) n0 = 28 * (nt >> 1) + 16 * (nt & 1);
    else if (TYPE == T_KV) n0 = (nt < 4 ? 0 : 56) + 28 * ((nt & 3) >> 1) + 16 * (nt & 1);
    else n0 = 16 * nt;
    int byte = 2 * (n0 + 4 * p4);
    int sw = q4 | ((g & 1) << 2);
    baddr[nt] = wn * 16384 + (8 * g + q4) * 256 + (((byte >> 4) ^ (sw << 1)) << 4) + (byte & 15);
  }
  int caddr0 = baddr[0], caddr1 = baddr[1];
#pragma unroll
  for (int w = 1; w < 4; ++w) {
    caddr0 = (wm == w) ? baddr[2 * w] : caddr0;
    caddr1 = (wm == w) ? baddr[(2 * w + 1 < NT) ? 2 * w + 1 : 2 * w] : caddr1;
  }
  const int xrow = wm * 32 + l15;
  const int xa0 = K1_XOFF + xrow * 128 + ((g ^ ((xrow >> 1) & 7)) << 4), xa1 = xa0 ^ 64;
  const int xs0 = xa0 + wn * 2048, xs1 = xa1 + wn * 2048;
  int baddrH[NT], caddr0H = caddr0 + 65536, caddr1H = caddr1 + 65536;
#pragma unroll
  for (int nt = 0; nt < NT; ++nt) { baddrH[nt] = baddr[nt] + 65536; asm volatile("" : "+v"(baddrH[nt])); }
  asm volatile("" : "+v"(caddr0H), "+v"(caddr1H));

  f32x4 acc[2][NT];
#pragma unroll
  for (int mt = 0; mt < 2; ++mt)
#pragma unroll
    for (int nt = 0; nt < NT; ++nt) acc[mt][nt] = f32x4{0.f, 0.f, 0.f, 0.f};
  f32x4 rs_ = {0.f, 0.f, 0.f, 0.f}, rq_ = rs_, cs0 = rs_, cs1 = rs_;
  const u32x4 ONES = {0x3f803f80u, 0x3f803f80u, 0x3f803f80u, 0x3f803f80u};

  struct Frags { u32x4 x0, x1, xs, c0, c1, b[NT]; };
  Frags fr[2];
  auto step = [&](const Frags& f, Frags& n, bool ld, int cn, int ksn, int sc) {
    const int st = cn % 3;
    const int wo = (st == 2 ? 0 : st * K1_WST) + ksn * 8192;
    const int xo = st * K1_XST;
    const int scc = sc < 0 ? 0 : sc;
    const unsigned dx = ldsb + K1_XOFF + (scc % 3) * K1_XST + wave * 1024;
    const unsigned dw = ldsb + (scc % 3) * K1_WST + wave * 1024;
    const char* xb = (const char*)x + scc * 128;
    const char* wb0 = Wh0 + (size_t)scc * 64 * ldw0;
    const char* wb1 = Wh1 + (size_t)scc * 64 * ldw1;
#pragma unroll
    for (int nt = 0; nt < NT; ++nt) {
      if (ld) {
        if (nt == 0) { n.x0 = *(lds_u4*)(lds + (ksn ? xa1 : xa0) + xo); n.x1 = *(lds_u4*)(lds + (ksn ? xa1 : xa0) + xo + 2048); }
        lds_char* bp = lds + (st == 2 ? baddrH[nt] : baddr[nt]) + wo;
        n.b[nt] = tr_read2(bp, bp + 1024);
        if (nt == 3) { lds_char* cp = lds + (st == 2 ? caddr0H : caddr0) + wo; n.c0 = tr_read2(cp, cp + 1024); }
        if (nt == 4) { lds_char* cp = lds + (st == 2 ? caddr1H : caddr1) + wo; n.c1 = tr_read2(cp, cp + 1024); }
        if (nt == 5) n.xs = *(lds_u4*)(lds + (ksn ? xs1 : xs0) + xo);
      }
      const bool trans = (TYPE == T_Q) || (TYPE == T_Z1) || (TYPE == T_KV && nt < 4);
      acc[0][nt] = trans ? mfma16(f.b[nt], f.x0, acc[0][nt]) : mfma16(f.x0, f.b[nt], acc[0][nt]);
      acc[1][nt] = trans ? mfma16(f.b[nt], f.x1, acc[1][nt]) : mfma16(f.x1, f.b[nt], acc[1][nt]);
      if (nt == 1) cs0 = mfma16(ONES, f.c0, cs0);
      if (nt == 2) cs1 = mfma16(ONES, f.c1, cs1);
      if (nt == 3) rs_ = mfma16(ONES, f.xs, rs_);
      if (nt == 4) rq_ = mfma16(f.xs, f.xs, rq_);
      if (sc >= 0 && nt < 6) {
        if (nt < 2) glds16(xb, xoff[nt], dx + nt * 8192);
        else glds16(nt < 4 ? wb0 : wb1, goff[nt - 2], dw + (nt - 2) * 8192);
      }
      __builtin_amdgcn_sched_barrier(0);
    }
  };
  WAIT_VM(12);
  BARRIER();
  {
    Frags& n = fr[0];
    n.x0 = *(lds_u4*)(lds + xa0); n.x1 = *(lds_u4*)(lds + xa0 + 2048);
#pragma unroll
    for (int nt = 0; nt < NT; ++nt) n.b[nt] = tr_read2(lds + baddr[nt], lds + baddr[nt] + 1024);
    n.c0 = tr_read2(lds + caddr0, lds + caddr0 + 1024);
    n.c1 = tr_read2(lds + caddr1, lds + caddr1 + 1024);
    n.xs = *(lds_u4*)(lds + xs0);
  }
#pragma unroll
  for (int c = 0; c < 7; ++c) {
    step(fr[0], fr[1], true, c, 1, -1);
    if (c < 6) {
      if (c < 5) WAIT_VM(6); else WAIT_VM(0);
      WAIT_LGKM0();
      BARRIER();
      if (c == 5 && TYPE == T_KV) {
        unsigned o0 = min(2 * hj * 1568 + wm * 1024 + lane * 16, 25088 - 16);
        glds16(wphi, o0, ldsb + K1_WPHI + wm * 1024);
      }
    }
    step(fr[1], fr[0], c < 6, c + 1, 0, (c + 3 < 7) ? c + 3 : -1);
  }

  lds_f* cstab = (lds_f*)(lds + K1_CS);
  lds_f* rtab = (lds_f*)(lds + K1_RT);
  lds_f* mtab = (lds_f*)(lds + K1_MT);
  {
    float s = rs_[0], c0 = cs0[0], c1 = cs1[0];
    float q = (p4 == 0) ? rq_[0] : (p4 == 1) ? rq_[1] : (p4 == 2) ? rq_[2] : rq_[3];
    q = (g == q4) ? q : 0.f;
    GRP_RED(q, aadd);
    float mean = s * (1.f / 448.f);
    float var = fmaxf(q * (1.f / 448.f) - mean * mean, 0.f);
    float mb = rbf(mean), vb = rbf(var);
    float sd = rbf(__builtin_amdgcn_sqrtf(rbf(vb + 1e-5f)));
    float r = frcp(sd);
    if (g == 0) {
      cstab[(wn * 8 + 2 * wm) * 16 + l15] = c0;
      cstab[(wn * 8 + 2 * wm + 1) * 16 + l15] = c1;
      rtab[wm * 32 + wn * 16 + l15] = r;
      mtab[wm * 32 + wn * 16 + l15] = -mb * r;
    }
  }
  WAIT_VM(0);
  WAIT_LGKM0();
  BARRIER();

  if (TYPE == T_Q || TYPE == T_KV || TYPE == T_Z1) {
    f32x2 Rr[2], Mr[2];
#pragma unroll
    for (int mt = 0; mt < 2; ++mt) {
      float r = rtab[wm * 32 + mt * 16 + l15], m = mtab[wm * 32 + mt * 16 + l15];
      Rr[mt] = f32x2{r, r}; Mr[mt] = f32x2{m, m};
    }
    if (TYPE == T_Z1) {
#pragma unroll
      for (int nt = 0; nt < NT; ++nt) {
        f32x4 c4 = *(lds_f4*)(cstab + (wn * 8 + nt) * 16 + 4 * g);
        const int f = hj * 112 + nt * 16 + 4 * g;
        const int R = (f & 31) >> 3, hh = (f >> 2) & 1;
#pragma unroll
        for (int mt = 0; mt < 2; ++mt) {
          f32x2 a = gelu2(pk_fma(lo2(c4), Mr[mt], lo2(acc[mt][nt]) * Rr[mt]));
          f32x2 b = gelu2(pk_fma(hi2(c4), Mr[mt], hi2(acc[mt][nt]) * Rr[mt]));
          const int s = row0 + wm * 32 + mt * 16 + l15;
          char* dst = ws + WS_Z1B + (size_t)((s >> 5) * 28 + (f >> 5)) * 2048 + R * 512 + ((s & 31) + 32 * hh) * 8;
          st8(dst, u32x2{pack2v(a), pack2v(b)});
        }
      }
      BARRIER();
    } else {
      constexpr int NHW = (TYPE == T_Q) ? 4 : 2;
      const bool inval = (g == 3);
      lds_char* scr = lds + wm * 16384;
      f32x4 pp[2][2][2];
#pragma unroll
      for (int hh = 0; hh < NHW; ++hh) {
        const int head = NHW * hj + hh;
        u32x4 a0, a1;
        if (TYPE == T_KV) {
          lds_char* wp = lds + K1_WPHI + hh * 1568 + (4 * g + q4) * 56 + 8 * p4;
          a0 = tr_read2(wp, wp + 16 * 56);
          a1 = tr_read2(wp + 32, wp + 16 * 56 + 32);
        }
        f32x4 c0 = *(lds_f4*)(cstab + (wn * 8 + 2 * hh) * 16 + 4 * g);
        f32x4 c1 = *(lds_f4*)(cstab + (wn * 8 + 2 * hh + 1) * 16 + 4 * g);
#pragma unroll
        for (int mt = 0; mt < 2; ++mt) {
          u32x4 bq;
          bq[0] = pack2v(pk_fma(lo2(c0), Mr[mt], lo2(acc[mt][2 * hh]) * Rr[mt]));
          bq[1] = pack2v(pk_fma(hi2(c0), Mr[mt], hi2(acc[mt][2 * hh]) * Rr[mt]));
          bq[2] = pack2v(pk_fma(lo2(c1), Mr[mt], lo2(acc[mt][2 * hh + 1]) * Rr[mt]));
          bq[3] = pack2v(pk_fma(hi2(c1), Mr[mt], hi2(acc[mt][2 * hh + 1]) * Rr[mt]));
          bq[2] = inval ? 0u : bq[2]; bq[3] = inval ? 0u : bq[3];
          if (TYPE == T_Q) {
            const int stile = rb * 8 + wm * 2 + mt;
            st16(ws + WS_PHIQ + (size_t)(head * 128 + stile) * 1024 + lane * 16, bq);
          } else {
            f32x4 z4 = {0.f, 0.f, 0.f, 0.f};
            pp[hh & 1][mt][0] = mfma16(a0, bq, z4); pp[hh & 1][mt][1] = mfma16(a1, bq, z4);
          }
        }
      }
      if (TYPE == T_KV) {
        u32x4 vb[2][2];
        f32x4 r4[2], m4[2];
#pragma unroll
        for (int mt = 0; mt < 2; ++mt) {
          r4[mt] = *(lds_f4*)(rtab + wm * 32 + mt * 16 + 4 * g);
          m4[mt] = *(lds_f4*)(mtab + wm * 32 + mt * 16 + 4 * g);
        }
#pragma unroll
        for (int hh = 0; hh < 2; ++hh)
#pragma unroll
          for (int dt = 0; dt < 2; ++dt) {
            const int nt = 4 + 2 * hh + dt;
            float csd = cstab[(wn * 8 + nt) * 16 + l15];
            const f32x2 cc = {csd, csd};
#pragma unroll
            for (int mt = 0; mt < 2; ++mt) {
              vb[hh][dt][2 * mt] = pack2v(pk_fma(cc, lo2(m4[mt]), lo2(acc[mt][nt]) * lo2(r4[mt])));
              vb[hh][dt][2 * mt + 1] = pack2v(pk_fma(cc, hi2(m4[mt]), hi2(acc[mt][nt]) * hi2(r4[mt])));
            }
            if (dt == 1) {
              const unsigned fill = (l15 == 12) ? 0x3f803f80u : 0u;
#pragma unroll
              for (int i = 0; i < 4; ++i) vb[hh][1][i] = (l15 >= 12) ? fill : vb[hh][1][i];
            }
          }
#pragma unroll
        for (int hh = 0; hh < 2; ++hh)
#pragma unroll
          for (int mt = 0; mt < 2; ++mt) {
            u32x4 pos, neg;
            hedgehog_softmax(pp[hh][mt][0], pp[hh][mt][1], inval, pos, neg);
            lds_char* d = scr + hh * 5120 + (mt * 16 + l15) * 160 + 8 * g;
            *(lds_u2*)(d) = u32x2{pos[0], pos[1]};
            *(lds_u2*)(d + 32) = u32x2{pos[2], pos[3]};
            *(lds_u2*)(d + 64) = u32x2{neg[0], neg[1]};
            *(lds_u2*)(d + 96) = u32x2{neg[2], neg[3]};
          }
        f32x4 kv[2][4][2];
#pragma unroll
        for (int hh = 0; hh < 2; ++hh) {
          lds_char* rp = scr + hh * 5120 + (4 * g + q4) * 160 + 8 * p4;
#pragma unroll
          for (int ft = 0; ft < 4; ++ft) {
            u32x4 pa = tr_read2(rp + ft * 32, rp + 16 * 160 + ft * 32);
            f32x4 z4 = {0.f, 0.f, 0.f, 0.f};
            kv[hh][ft][0] = mfma16(pa, vb[hh][0], z4);
            kv[hh][ft][1] = mfma16(pa, vb[hh][1], z4);
          }
        }
        WAIT_LGKM0();
#pragma unroll
        for (int hh = 0; hh < 2; ++hh) {
          lds_char* q = scr + (hh == 0 ? 8192 : 0) + lane * 16;
#pragma unroll
          for (int ft = 0; ft < 4; ++ft) {
            *(lds_f4*)(q + (ft * 2 + 0) * 1024) = kv[hh][ft][0];
            *(lds_f4*)(q + (ft * 2 + 1) * 1024) = kv[hh][ft][1];
          }
        }
      }
      WAIT_LGKM0();
      BARRIER();
      if (TYPE == T_KV) {
#pragma unroll
        for (int it = 0; it < 4; ++it) {
          int pos = it * 256 + tid;
          f32x4 a = *(lds_f4*)(lds + 0 * 16384 + pos * 16);
          a += *(lds_f4*)(lds + 1 * 16384 + pos * 16);
          a += *(lds_f4*)(lds + 2 * 16384 + pos * 16);
          a += *(lds_f4*)(lds + 3 * 16384 + pos * 16);
          int head = 2 * hj + 1 - (pos >> 9);
          st16(ws + WS_KVS + ((size_t)(head * 16 + rb) * 8) * 1024 + (pos & 511) * 16, __builtin_bit_cast(u32x4, a));
        }
      }
    }
  } else {
    lds_f* st = (lds_f*)(lds + K1_ST2);
#pragma unroll
    for (int mt = 0; mt < 2; ++mt) {
      f32x4 r4 = *(lds_f4*)(rtab + wm * 32 + mt * 16 + 4 * g);
      f32x4 m4 = *(lds_f4*)(mtab + wm * 32 + mt * 16 + 4 * g);
      f32x2 sa = {0.f, 0.f}, sb = {0.f, 0.f}, qa = {0.f, 0.f}, qb = {0.f, 0.f};
#pragma unroll
      for (int nt = 0; nt < NT; ++nt) {
        float csd = cstab[(wn * 8 + nt) * 16 + l15];
        const f32x2 cc = {csd, csd};
        f32x2 a = gelu2(pk_fma(cc, lo2(m4), lo2(acc[mt][nt]) * lo2(r4)));
        f32x2 b = gelu2(pk_fma(cc, hi2(m4), hi2(acc[mt][nt]) * hi2(r4)));
        unsigned pa = pack2v(a), pb = pack2v(b);
        sa += a; sb += b; qa = pk_fma(a, a, qa); qb = pk_fma(b, b, qb);
        const int f = hj * 112 + nt * 16 + l15;
        const int s16 = rb * 8 + wm * 2 + mt;
        char* dst = ws + WS_Z2B + (size_t)((f >> 5) * 128 + s16) * 1024 + ((f & 31) + 32 * (g >> 1)) * 16 + (g & 1) * 8;
        st8(dst, u32x2{pa, pb});
      }
      f32x4 o0 = {row_sum16(sa[0]), row_sum16(qa[0]), row_sum16(sa[1]), row_sum16(qa[1])};
      f32x4 o1 = {row_sum16(sb[0]), row_sum16(qb[0]), row_sum16(sb[1]), row_sum16(qb[1])};
      if (l15 == 0) {
        *(lds_f4*)(st + (wn * 128 + wm * 32 + mt * 16 + 4 * g) * 2) = o0;
        *(lds_f4*)(st + (wn * 128 + wm * 32 + mt * 16 + 4 * g) * 2 + 4) = o1;
      }
    }
    WAIT_LGKM0();
    BARRIER();
    if (tid < 128) {
      f32x2 a = *(lds_f2*)(st + tid * 2), b = *(lds_f2*)(st + (128 + tid) * 2);
      *(f32x2*)(ws + WS_STAT + ((size_t)(cg - 10) * 2048 + row0 + tid) * 8) = a + b;
    }
  }
}

__global__ __launch_bounds__(512, 2) void k1_kernel(const __bf16* __restrict__ x, const __bf16* __restrict__ wqkv,
                                                    const __bf16* __restrict__ wphi, const __bf16* __restrict__ win,
                                                    const __bf16* __restrict__ wsp, const __bf16* __restrict__ wo,
                                                    const __bf16* __restrict__ wout, char* __restrict__ ws) {
  extern __shared__ __attribute__((aligned(16))) char smem[];
  lds_char* lds = (lds_char*)smem;
  const int b = blockIdx.x;
  const int xcd = b & 7, idx = b >> 3;
  const int tid = threadIdx.x, lane = tid & 63;
  if (idx >= 28) {
    const int e = xcd * 4 + (idx - 28);
    unsigned acc = 0;
    const u32x4* p = (const u32x4*)wsp + (size_t)e * 16384 + tid;
#pragma unroll 8
    for (int i = 0; i < 32; ++i) { u32x4 v = p[i * 512]; acc ^= v[0] ^ v[3]; }
    const u32x4* q = (const u32x4*)wout + e * 1568 + tid;
#pragma unroll
    for (int i = 0; i < 4; ++i) if (i * 512 + tid < 1568) { u32x4 v = q[i * 512]; acc ^= v[0] ^ v[3]; }
    const u32x4* r = (const u32x4*)wo + e * 784 + tid;
#pragma unroll
    for (int i = 0; i < 2; ++i) if (i * 512 + tid < 784) { u32x4 v = r[i * 512]; acc ^= v[0] ^ v[3]; }
    asm volatile("" :: "v"(acc));
    return;
  }
  const int rb = 2 * xcd + idx / 14, cg = idx % 14;
  const int wave = __builtin_amdgcn_readfirstlane(tid >> 6);
  const unsigned ldsb = (unsigned)(size_t)lds;
  unsigned goff[4], xoff[2];
#pragma unroll
  for (int i = 0; i < 2; ++i) {
    int row = (i * 8 + wave) * 8 + (lane >> 3);
    int c = (lane & 7) ^ ((row >> 1) & 7);
    xoff[i] = (rb * 128 + row) * 896 + c * 16;
  }
  glds16(x, xoff[0], ldsb + K1_XOFF + wave * 1024);
  glds16(x, xoff[1], ldsb + K1_XOFF + wave * 1024 + 8192);
  int ht[2], hj[2];
  k1_half(cg, 0, ht[0], hj[0]);
  k1_half(cg, 1, ht[1], hj[1]);
  const char* Wh[2]; int ldw[2];
#pragma unroll
  for (int h = 0; h < 2; ++h) {
    const bool qkv = ht[h] <= T_KV;
    Wh[h] = uni_ptr(qkv ? (const char*)wqkv : (const char*)win);
    ldw[h] = __builtin_amdgcn_readfirstlane(qkv ? 2688 : 3584);
    const int colA = (ht[h] == T_KV) ? 896 + 112 * hj[h] : 224 * hj[h] + (ht[h] == T_Z2 ? 1792 : 0);
    const int delta = (ht[h] == T_KV) ? 784 : 0, lim = (ht[h] == T_Q) ? 15 : 14;
#pragma unroll
    for (int i = 0; i < 2; ++i) {
      int row = (i * 8 + wave) * 4 + (lane >> 4);
      int sw = (row & 3) | (((row >> 3) & 1) << 2);
      int c = (lane & 15) ^ (sw << 1);
      c = c < lim ? c : 0;
      goff[h * 2 + i] = row * ldw[h] + colA + 16 * c + (c >= 7 ? delta : 0);
    }
  }
#pragma unroll
  for (int chunk = 0; chunk < 3; ++chunk) {
    const unsigned dx = ldsb + K1_XOFF + chunk * K1_XST + wave * 1024;
    const unsigned dw = ldsb + chunk * K1_WST + wave * 1024;
    const char* xb = (const char*)x + chunk * 128;
    if (chunk > 0) {
      glds16(xb, xoff[0], dx);
      glds16(xb, xoff[1], dx + 8192);
    }
#pragma unroll
    for (int i = 0; i < 4; ++i) glds16(Wh[i >> 1] + (size_t)chunk * 64 * ldw[i >> 1], goff[i], dw + i * 8192);
  }
  const int my = (wave >> 2) ? ht[1] : ht[0], mj = (wave >> 2) ? hj[1] : hj[0];
  if (my == T_Q) k1_wave<T_Q>(x, Wh[0], Wh[1], ldw[0], ldw[1], goff, xoff, wphi, ws, lds, rb, cg, mj);
  else if (my == T_KV) k1_wave<T_KV>(x, Wh[0], Wh[1], ldw[0], ldw[1], goff, xoff, wphi, ws, lds, rb, cg, mj);
  else if (my == T_Z1) k1_wave<T_Z1>(x, Wh[0], Wh[1], ldw[0], ldw[1], goff, xoff, wphi, ws, lds, rb, cg, mj);
  else k1_wave<T_Z2>(x, Wh[0], Wh[1], ldw[0], ldw[1], goff, xoff, wphi, ws, lds, rb, cg, mj);
}

constexpr int K2_WB = 0;
constexpr int K2_RT = 32768;
constexpr int K2_MT = K2_RT + 8192;
constexpr int K2_CT = K2_MT + 4096;
constexpr int K2_LDS = K2_CT + 512;

__global__ __launch_bounds__(768, 3) void k2_kernel(const __bf16* __restrict__ wsp, const __bf16* __restrict__ bsp,
                                                    const __bf16* __restrict__ wphi, char* __restrict__ ws) {
  extern __shared__ __attribute__((aligned(16))) char smem[];
  lds_char* lds = (lds_char*)smem;
  const int tid = threadIdx.x, lane = tid & 63;
  const int wave = __builtin_amdgcn_readfirstlane(tid >> 6);
  const int b = blockIdx.x, xcd = b & 7, idx = b >> 3;
  if (idx >= 28) {
    if (wave >= 8) return;
    const int e = xcd * 4 + (idx - 28), head = e >> 1, half = e & 1;
    const int g = lane >> 4, l15 = lane & 15, q4 = (lane >> 2) & 3, p4 = lane & 3;
    u32x4 qf[8];
    const char* pbase = ws + WS_PHIQ + (size_t)(head * 128 + half * 64 + wave * 8) * 1024 + lane * 16;
#pragma unroll
    for (int i = 0; i < 8; ++i) qf[i] = *(const u32x4*)(pbase + i * 1024);
    if (tid < 128) {
      const unsigned o = min(head * 1568 + tid * 16, 25088 - 16);
      *(lds_u4*)(lds + 8192 + tid * 16) = *(const u32x4*)((const char*)wphi + o);
    }
    {
      const char* kb = ws + WS_KVS + ((size_t)(head * 16) * 8 + wave) * 1024 + lane * 16;
      f32x4 a = {0.f, 0.f, 0.f, 0.f};
#pragma unroll
      for (int r = 0; r < 16; ++r) a += *(const f32x4*)(kb + (size_t)r * 8192);
      *(lds_u2*)(lds + wave * 512 + lane * 8) = u32x2{pack2(a[0], a[1]), pack2(a[2], a[3])};
    }
    __syncthreads();
    u32x4 af[2][2];
#pragma unroll
    for (int sg = 0; sg < 2; ++sg)
#pragma unroll
      for (int dt = 0; dt < 2; ++dt) {
        u32x2 lo = *(lds_u2*)(lds + ((2 * sg) * 2 + dt) * 512 + lane * 8);
        u32x2 hi = *(lds_u2*)(lds + ((2 * sg + 1) * 2 + dt) * 512 + lane * 8);
        af[sg][dt] = u32x4{lo[0], lo[1], hi[0], hi[1]};
      }
    lds_char* wp = lds + 8192 + (4 * g + q4) * 56 + 8 * p4;
    const u32x4 a0 = tr_read2(wp, wp + 16 * 56), a1 = tr_read2(wp + 32, wp + 16 * 56 + 32);
    const bool inval = (g == 3);
#pragma unroll
    for (int i = 0; i < 8; ++i) {
      f32x4 z4 = {0.f, 0.f, 0.f, 0.f};
      f32x4 p0 = mfma16(a0, qf[i], z4), p1 = mfma16(a1, qf[i], z4);
      u32x4 pos, neg;
      hedgehog_softmax(p0, p1, inval, pos, neg);
      f32x4 n0 = mfma16(af[0][0], pos, z4); n0 = mfma16(af[1][0], neg, n0);
      f32x4 n1 = mfma16(af[0][1], pos, z4); n1 = mfma16(af[1][1], neg, n1);
      float den = __shfl(n1[0], 48 + l15);
      den = rbf(rbf(den) + 1e-6f);
      float rd = 1.f / den;
      const int s = half * 1024 + (wave * 8 + i) * 16 + l15;
#pragma unroll
      for (int dt = 0; dt < 2; ++dt) {
        const f32x4& n = dt ? n1 : n0;
        u32x2 o = {pack2(rbf(n[0]) * rd, rbf(n[1]) * rd), pack2(rbf(n[2]) * rd, rbf(n[3]) * rd)};
        const int k = 28 * head + 16 * dt + 4 * g;
        const int kl = k & 15, hh = (kl >> 2) & 1, j0 = (kl >> 3) * 4;
        char* dst = ws + WS_HO + (size_t)((s >> 5) * 84 + (k >> 4)) * 1024 + ((s & 31) + 32 * hh) * 16 + j0 * 2;
        if (!(dt == 1 && g == 3)) st8(dst, o);
      }
    }
    return;
  }

  const int rb = 4 * xcd + idx / 7, ct = idx % 7;
  constexpr int NK = 16;
  const int koff = (ct * 2 + (ct > 3 ? 1 : 0)) & 15;
#define KROT(k) ((((k) + koff) & 15))
  auto stat_load = [&](int row, f32x2 (&a)[4]) {
#pragma unroll
    for (int gq = 0; gq < 4; ++gq) a[gq] = *(const f32x2*)(ws + WS_STAT + ((size_t)gq * 2048 + row) * 8);
  };
  auto stat_put = [&](int row, const f32x2 (&a)[4]) {
    f32x2 t = (a[0] + a[1]) + (a[2] + a[3]);
    float mean = t[0] * (1.f / 896.f);
    float var = fmaxf(t[1] * (1.f / 896.f) - mean * mean, 0.f);
    float vb = rbf(var);
    float sd = rbf(__builtin_amdgcn_sqrtf(rbf(vb + 1e-5f)));
    *(lds_f*)(lds + K2_RT + row * 4) = frcp(sd);
    *(__attribute__((address_space(3))) unsigned short*)(lds + K2_MT + row * 2) = (unsigned short)pack2(mean, 0.f);
  };
  const int srow = KROT(tid >> 7) * 128 + (tid & 127);
  f32x2 sa0[4];
  if (wave >= 4) {
    __builtin_amdgcn_s_setprio(2);
    const int pt = tid - 256, t = pt >> 4, cp = pt & 15;
    const int g = lane >> 4, l15 = lane & 15;
    const char* src = (const char*)wsp + (size_t)(rb * 64 + t) * 4096 + cp * 16;
    constexpr int PD = 4;
    u32x4 wr[PD][2];
#define WLOAD(dst, kk) do { _Pragma("unroll") for (int j = 0; j < 2; ++j) \
      dst[j] = *(const u32x4*)(src + KROT(kk) * 256 + j * (32 * 4096)); } while (0)
    WLOAD(wr[0], 0);
    if (tid < 512) {
      stat_load(srow, sa0);
      __builtin_amdgcn_sched_barrier(0);
      stat_put(srow, sa0);
    }
    const int wa = t * 256 + ((cp ^ (t & 15)) << 4);
    const int ta = cp * 32;
    const int pw = wave - 4;
    const int crow = (pw >> 1) * 16 + l15;
    const int ca = (crow * 256 + ((g ^ l15) << 4)) ^ ((pw & 1) << 7);
    f32x4 cacc = {0.f, 0.f, 0.f, 0.f};
    WAIT_LGKM0(); BARRIER();
    f32x4 rr[2][2];
    rr[0][0] = *(lds_f4*)(lds + K2_RT + KROT(0) * 512 + ta); rr[0][1] = *(lds_f4*)(lds + K2_RT + KROT(0) * 512 + ta + 16);
    WLOAD(wr[1], 1);
    f32x2 sb[3][4];
    __builtin_amdgcn_sched_barrier(0);
#pragma unroll
    for (int k = 0; k <= NK; ++k) {
      if (k == 1) {
#pragma unroll
        for (int i = 0; i < 3; ++i) stat_load(KROT(4 + 4 * i + (pt >> 7)) * 128 + (pt & 127), sb[i]);
      }
      lds_char* wb = lds + K2_WB + (k & 1) * 16384;
      lds_char* pb = lds + K2_WB + ((k + 1) & 1) * 16384;
      u32x4 mf[2], wf[2];
#pragma unroll
      for (int j = 0; j < 2; ++j) {
        if (k > 0) {
          mf[j] = *(lds_u4*)(lds + K2_MT + (KROT(k - 1) * 128 + ((pw & 1) * 2 + j) * 32 + g * 8) * 2);
          wf[j] = *(lds_u4*)(pb + (ca ^ (j << 6)));
        }
      }
      if (k + 1 < NK) {
        rr[(k + 1) & 1][0] = *(lds_f4*)(lds + K2_RT + KROT(k + 1) * 512 + ta);
        rr[(k + 1) & 1][1] = *(lds_f4*)(lds + K2_RT + KROT(k + 1) * 512 + ta + 16);
      }
      __builtin_amdgcn_sched_barrier(0);
#pragma unroll
      for (int j = 0; j < 2; ++j) {
        if (k < NK) {
          const f32x4 r0 = rr[k & 1][0], r1 = rr[k & 1][1];
          u32x4 w = wr[k % PD][j];
          if (k + PD < NK) wr[k % PD][j] = *(const u32x4*)(src + KROT(k + PD) * 256 + j * (32 * 4096));
          u32x4 o;
          o[0] = pack2(smul(bf_lo(w[0]), r0[0]), smul(bf_hi(w[0]), r0[1]));
          o[1] = pack2(smul(bf_lo(w[1]), r0[2]), smul(bf_hi(w[1]), r0[3]));
          o[2] = pack2(smul(bf_lo(w[2]), r1[0]), smul(bf_hi(w[2]), r1[1]));
          o[3] = pack2(smul(bf_lo(w[3]), r1[2]), smul(bf_hi(w[3]), r1[3]));
          *(lds_u4*)(wb + wa + j * 8192) = o;
        }
        __builtin_amdgcn_sched_barrier(0);
        if (k > 0) cacc = mfma16(mf[j], wf[j], cacc);
      }
      if (k == 0) { WLOAD(wr[2], 2); WLOAD(wr[3], 3); }
      if (k == 2) {
#pragma unroll
        for (int i = 0; i < 3; ++i) stat_put(KROT(4 + 4 * i + (pt >> 7)) * 128 + (pt & 127), sb[i]);
      }
      if (k == NK) {
        if (g == 0) *(lds_f*)(lds + K2_CT + ((pw & 1) * 64 + crow) * 4) = cacc[0];
      }
      WAIT_LGKM0();
      BARRIER();
    }
  } else {
    const int F = 4 * ct + wave, l31 = lane & 31, hh = lane >> 5;
    const char* zsrc = ws + WS_Z2B + (size_t)F * 128 * 1024 + lane * 16;
    constexpr int ZD = 16;
    u32x4 zf[ZD];
#pragma unroll
    for (int i = 0; i < 8; ++i) zf[i] = *(const u32x4*)(zsrc + (KROT(i >> 3) * 8 + (i & 7)) * 1024);
    stat_load(srow, sa0);
    __builtin_amdgcn_sched_barrier(0);
    stat_put(srow, sa0);
    WAIT_LGKM0(); BARRIER();
    const int ra = l31 * 256 + ((hh ^ (l31 & 15)) << 4);
    f32x16 acc[2];
#pragma unroll
    for (int i = 0; i < 16; ++i) { acc[0][i] = 0.f; acc[1][i] = 0.f; }
    u32x2 z1v[2][4];
    float bb[2];
    BARRIER();
    int rk[8];
#pragma unroll
    for (int kk = 0; kk < 8; ++kk) { rk[kk] = ra ^ (kk << 5); }
    constexpr int LD = 3;
    u32x4 wq[LD + 1][2];
#pragma unroll
    for (int i = 0; i < LD; ++i) { wq[i][0] = *(lds_u4*)(lds + rk[i]); wq[i][1] = *(lds_u4*)(lds + rk[i] + 8192); }
#pragma unroll
    for (int k = 0; k < NK; ++k) {
#pragma unroll
      for (int kk = 0; kk < 8; ++kk) {
        const int ks = k * 8 + kk;
        if (kk == 8 - LD && k + 1 < NK) { WAIT_LGKM0(); BARRIER(); }
        if (ks + LD < 128) {
          const int nb = (((ks + LD) >> 3) & 1) * 16384;
          wq[(ks + LD) % (LD + 1)][0] = *(lds_u4*)(lds + rk[(kk + LD) & 7] + nb);
          wq[(ks + LD) % (LD + 1)][1] = *(lds_u4*)(lds + rk[(kk + LD) & 7] + nb + 8192);
        }
        const u32x4 w0 = wq[ks % (LD + 1)][0], w1 = wq[ks % (LD + 1)][1];
        if (ks == 108) {
#pragma unroll
          for (int tt = 0; tt < 2; ++tt) {
            const int t = rb * 64 + tt * 32 + l31;
            bb[tt] = bf_lo((unsigned)*(const unsigned short*)(bsp + t));
            const char* z1p = ws + WS_Z1B + (size_t)((t >> 5) * 28 + F) * 2048 + lane * 8;
#pragma unroll
            for (int R = 0; R < 4; ++R) z1v[tt][R] = *(const u32x2*)(z1p + R * 512);
          }
        }
        u32x4 z = zf[ks % ZD];
#define ZLOAD(i) zf[(i) % ZD] = *(const u32x4*)(zsrc + (KROT((i) >> 3) * 8 + ((i) & 7)) * 1024)
        if (ks < ZD - 8) { ZLOAD(8 + 2 * ks); ZLOAD(9 + 2 * ks); }
        else if (ks + ZD < 128) ZLOAD(ks + ZD);
        acc[0] = mfma32(z, w0, acc[0]);
        acc[1] = mfma32(z, w1, acc[1]);
        __builtin_amdgcn_sched_barrier(0);
      }
    }
    WAIT_LGKM0();
    BARRIER();
#pragma unroll
    for (int tt = 0; tt < 2; ++tt) {
      const int t = rb * 64 + tt * 32 + l31;
      const float c = *(lds_f*)(lds + K2_CT + (tt * 32 + l31) * 4) + *(lds_f*)(lds + K2_CT + (64 + tt * 32 + l31) * 4);
      const f32x2 cc = {c, c}, b2 = {bb[tt], bb[tt]};
      unsigned hp[8];
#pragma unroll
      for (int R = 0; R < 4; ++R) {
        f32x2 g0 = unpack2(pack2v(f32x2{acc[tt][4 * R + 0], acc[tt][4 * R + 1]} - cc));
        f32x2 g1 = unpack2(pack2v(f32x2{acc[tt][4 * R + 2], acc[tt][4 * R + 3]} - cc));
        g0 = unpack2(pack2v(g0 + b2)); g1 = unpack2(pack2v(g1 + b2));
        hp[2 * R] = pack2v(unpack2(z1v[tt][R][0]) * g0); hp[2 * R + 1] = pack2v(unpack2(z1v[tt][R][1]) * g1);
      }
      char* dst = ws + WS_HO + (size_t)((t >> 5) * 84 + 28 + 2 * F) * 1024 + lane * 16;
      st16(dst, u32x4{hp[0], hp[1], hp[2], hp[3]});
      st16(dst + 1024, u32x4{hp[4], hp[5], hp[6], hp[7]});
    }
  }
}

constexpr int K3_STG = 16384;
constexpr int K3_NB = 5;
constexpr int K3_LDS = K3_STG * K3_NB;

__global__ __launch_bounds__(256, 2) void k3_kernel(const __bf16* __restrict__ x, const __bf16* __restrict__ wo,
                                                    const __bf16* __restrict__ wout, char* __restrict__ ws,
                                                    __bf16* __restrict__ out) {
  extern __shared__ __attribute__((aligned(16))) char smem[];
  lds_char* lds = (lds_char*)smem;
  const unsigned ldsb = (unsigned)(size_t)lds;
  const int tid = threadIdx.x, lane = tid & 63;
  const int wave = __builtin_amdgcn_readfirstlane(tid >> 6);
  const int b = blockIdx.x, xcd = b & 7, idx = b >> 3;
  const int rb = 4 * xcd + idx / 7, ct = idx % 7;
  const int wt = wave & 1, wn = wave >> 1;
  const int l31 = lane & 31, hh = lane >> 5, gi = lane >> 4, q4 = (lane >> 2) & 3, p4 = lane & 3;

  unsigned goff[2];
#pragma unroll
  for (int i = 0; i < 2; ++i) {
    int row = (i * 4 + wave) * 8 + (lane >> 3);
    int sw = ((row & 3) << 1) ^ (((row >> 3) & 1));
    int c = (lane & 7) ^ sw;
    goff[i] = row * 896 + ct * 128 + c * 16;
  }
  const char* abase = ws + WS_HO + (size_t)(rb * 2) * 84 * 1024;
  const unsigned aoff = wave * 1024 + lane * 16;
  auto stage = [&](int chunk) {
    const char* base = (chunk < 7) ? ((const char*)wo + (size_t)chunk * 64 * 896) : ((const char*)wout + (size_t)(chunk - 7) * 64 * 896);
    const unsigned dst = ldsb + (chunk % K3_NB) * K3_STG + wave * 1024;
    glds16(abase + chunk * 4096, aoff, dst);
    glds16(abase + chunk * 4096 + 84 * 1024, aoff, dst + 4096);
    glds16(base, goff[0], dst + 8192);
    glds16(base, goff[1], dst + 8192 + 4096);
  };
  const int t = rb * 64 + wt * 32 + l31;
  u32x2 xv[4];
  {
    const unsigned xo = (unsigned)(t * D_ + ct * 64 + wn * 32 + 4 * hh) * 2;
    asm volatile("global_load_dwordx2 %0, %4, %5\n\tglobal_load_dwordx2 %1, %4, %5 offset:16\n\t"
                 "global_load_dwordx2 %2, %4, %5 offset:32\n\tglobal_load_dwordx2 %3, %4, %5 offset:48"
                 : "=&v"(xv[0]), "=&v"(xv[1]), "=&v"(xv[2]), "=&v"(xv[3]) : "v"(xo), "s"(x) : "memory");
  }
  stage(0); stage(1); stage(2); stage(3);

  int baddr;
  {
    int row = 4 * (gi >> 1) + q4;
    int byte = 2 * (wn * 32 + (gi & 1) * 16 + 4 * p4);
    int sw = ((row & 3) << 1);
    baddr = 8192 + row * 128 + (((byte >> 4) ^ sw) << 4) + (byte & 15);
  }
  const int baddr2 = (baddr ^ 16) + 8 * 128;
  const int aaddr = wt * 4096 + lane * 16;

  f32x16 acc;
#pragma unroll
  for (int i = 0; i < 16; ++i) acc[i] = 0.f;
  unsigned t1[8];
  asm volatile("s_waitcnt vmcnt(12)" : "+v"(xv[0]), "+v"(xv[1]), "+v"(xv[2]), "+v"(xv[3]) :: "memory");
  BARRIER();
  constexpr int LD = 3;
  u32x4 wq[LD + 1], aq[LD + 1];
#pragma unroll
  for (int i = 0; i < LD; ++i) { wq[i] = tr_read2(lds + baddr + i * 2048, lds + baddr2 + i * 2048); aq[i] = *(lds_u4*)(lds + aaddr + i * 1024); }
#pragma unroll
  for (int c = 0; c < 21; ++c) {
#pragma unroll
    for (int kk = 0; kk < 4; ++kk) {
      const int ks = c * 4 + kk;
      if (kk == 4 - LD && c + 1 < 21) {
        if (c + 3 < 21) WAIT_VM(8); else if (c + 2 < 21) WAIT_VM(4); else WAIT_VM(0);
        WAIT_LGKM0();
        BARRIER();
        if (c + 4 < 21) stage(c + 4);
      }
      if (ks + LD < 84) {
        const int cn = (ks + LD) >> 2, kn = (ks + LD) & 3;
        lds_char* bufp = lds + (cn % K3_NB) * K3_STG;
        wq[(ks + LD) % (LD + 1)] = tr_read2(bufp + baddr + kn * 2048, bufp + baddr2 + kn * 2048);
        aq[(ks + LD) % (LD + 1)] = *(lds_u4*)(bufp + aaddr + kn * 1024);
      }
      acc = mfma32(wq[ks % (LD + 1)], aq[ks % (LD + 1)], acc);
      __builtin_amdgcn_sched_barrier(0);
    }
    if (c == 6) {
#pragma unroll
      for (int R = 0; R < 4; ++R) {
        f32x2 a0 = unpack2(pack2v(f32x2{acc[4 * R + 0], acc[4 * R + 1]}));
        f32x2 a1 = unpack2(pack2v(f32x2{acc[4 * R + 2], acc[4 * R + 3]}));
        t1[2 * R] = pack2v(unpack2(xv[R][0]) + a0); t1[2 * R + 1] = pack2v(unpack2(xv[R][1]) + a1);
      }
#pragma unroll
      for (int i = 0; i < 16; ++i) acc[i] = 0.f;
    }
  }
  __bf16* orow = out + (size_t)t * D_ + ct * 64 + wn * 32 + 4 * hh;
#pragma unroll
  for (int R = 0; R < 4; ++R) {
    f32x2 a0 = unpack2(pack2v(f32x2{acc[4 * R + 0], acc[4 * R + 1]}));
    f32x2 a1 = unpack2(pack2v(f32x2{acc[4 * R + 2], acc[4 * R + 3]}));
    *(u32x2*)(orow + 8 * R) = u32x2{pack2v(unpack2(t1[2 * R]) + a0), pack2v(unpack2(t1[2 * R + 1]) + a1)};
  }
}

extern "C" void kernel_launch(void* const* d_in, const int* in_sizes, int n_in, void* d_out, int out_size,
                              void* d_ws, size_t ws_size, hipStream_t stream) {
  const __bf16* x = (const __bf16*)d_in[0];
  const __bf16* wqkv = (const __bf16*)d_in[1];
  const __bf16* wphi = (const __bf16*)d_in[2];
  const __bf16* wo = (const __bf16*)d_in[3];
  const __bf16* win = (const __bf16*)d_in[5];
  const __bf16* wsp = (const __bf16*)d_in[6];
  const __bf16* bsp = (const __bf16*)d_in[7];
  const __bf16* wout = (const __bf16*)d_in[8];
  char* ws = (char*)d_ws;
  (void)hipFuncSetAttribute((const void*)k1_kernel, hipFuncAttributeMaxDynamicSharedMemorySize, K1_LDS);
  (void)hipFuncSetAttribute((const void*)k3_kernel, hipFuncAttributeMaxDynamicSharedMemorySize, K3_LDS);
  k1_kernel<<<256, 512, K1_LDS, stream>>>(x, wqkv, wphi, win, wsp, wo, wout, ws);
  k2_kernel<<<256, 768, K2_LDS, stream>>>(wsp, bsp, wphi, ws);
  k3_kernel<<<224, 256, K3_LDS, stream>>>(x, wo, wout, ws, (__bf16*)d_out);
}
```
